```python
import torch

def _softmax(x, axis=-1):
    return torch.softmax(x, dim=axis)
def _gelu(x):
    return torch.nn.functional.gelu(x, approximate="tanh")
def _silu(x):
    return torch.nn.functional.silu(x)
def _sigmoid(x):
    return torch.sigmoid(x)
def _layernorm(x, axis=-1, eps=1e-5):
    m = x.mean(dim=axis, keepdim=True)
    v = x.var(dim=axis, keepdim=True, unbiased=False)
    return (x - m) / torch.sqrt(v + eps)
def _scatter_add(tgt, idx, src, axis=0):
    return tgt.clone().index_add_(axis, idx, src)
def _segment_sum(src, seg, n):
    out = torch.zeros((n,) + src.shape[1:], dtype=src.dtype)
    return out.index_add_(0, seg, src)
def _pad(x, pw):
    flat = tuple(v for pair in reversed(pw) for v in pair)
    return torch.nn.functional.pad(x, flat)

kernel_name = 'gated_mix__linear_attn_block__mixer_token_mlp__prompt'

def setup_inputs():

    _w = (lambda a: (lambda t: t.to(torch.bfloat16) if t.is_floating_point() else t)(torch.as_tensor(a)))
    def _A_setup():
        B, S, D = 1, 2048, 512
        return {
            "x": _w((lambda *s: torch.randn(*s, dtype=torch.bfloat16) * 0.05)(B, S, D)),
            "wqkv": _w((lambda *s: torch.randn(*s, dtype=torch.bfloat16) * 0.4)(D, 3 * D)),
            "wo": _w((lambda *s: torch.randn(*s, dtype=torch.bfloat16) * 0.05)(D, D)),
        }
    _A_in = _A_setup()

    def _B_setup():
        B, S, D, F = 1, 2048, 512, 1792
        return {
            "x": _w((lambda *s: torch.randn(*s, dtype=torch.bfloat16) * 0.05)(B, S, D)),
            "w1": _w((lambda *s: torch.randn(*s, dtype=torch.bfloat16) * 0.4)(S, F) * (S ** -0.5)),
            "b1": _w((lambda *s: torch.randn(*s, dtype=torch.bfloat16) * 3.2)(F) * 0.02),
            "w2": _w((lambda *s: torch.randn(*s, dtype=torch.bfloat16) * 0.05)(F, S) * (F ** -0.5)),
            "b2": _w((lambda *s: torch.randn(*s, dtype=torch.bfloat16) * 0.1)(S) * 0.02),
        }
    _B_in = _B_setup()

    return {
        "A_x": _A_in["x"],
        "A_wqkv": _A_in["wqkv"],
        "A_wo": _A_in["wo"],
        "B_x": _B_in["x"],
        "B_w1": _B_in["w1"],
        "B_b1": _B_in["b1"],
        "B_w2": _B_in["w2"],
        "B_b2": _B_in["b2"],
        "gate_w": _w((lambda *s: torch.randn(*s, dtype=torch.bfloat16) * 0.05)(512) * 16.0),
    }

def reference(A_x, A_wqkv, A_wo, B_x, B_w1, B_b1, B_w2, B_b2, gate_w):
    def _A_ref(x, wqkv, wo):
        B, S, D = x.shape
        H = 32; Dh = D // H
        qkv = (lambda x, *d: x.permute(*d))((x @ wqkv).reshape(B, S, 3, H, Dh), 2, 0, 3, 1, 4)
        q, k, v = qkv[0], qkv[1], qkv[2]
        phi = lambda t: torch.where(t > 0, t + 1, torch.exp(t)) + 1e-6
        pq, pk = phi(q), phi(k)
        kv = torch.einsum("bhsd,bhse->bhde", pk, v)
        z = torch.einsum("bhsd->bhd", pk)
        num = torch.einsum("bhqd,bhde->bhqe", pq, kv)
        den = torch.einsum("bhqd,bhd->bhq", pq, z)[..., None] + 1e-6
        o = num / den
        return (lambda x, *d: x.permute(*d))(o, 0, 2, 1, 3).reshape(B, S, D) @ wo

    def _B_ref(x, w1, b1, w2, b2):
        B, S, D = x.shape
        h = _layernorm(x, axis=-1)
        u = torch.einsum("bsd,sf->bdf", h, w1) + b1
        u = _gelu(u)
        y = torch.einsum("bdf,fs->bds", u, w2) + b2
        return (lambda x, *d: x.permute(*d))(y, 0, 2, 1)

    x = A_x
    g = _sigmoid((x * gate_w).sum(-1, keepdims=True))
    a = _A_ref(x, A_wqkv, A_wo)
    b = _B_ref(x, B_w1, B_b1, B_w2, B_b2)
    return g * a + (1 - g) * b
```

```cpp
#define CODEPF 30720
#include <hip/hip_runtime.h>
#include <hip/hip_bf16.h>
#include <stdint.h>
#include <type_traits>

typedef __attribute__((ext_vector_type(8))) __bf16 bf16x8;
typedef __attribute__((ext_vector_type(2))) __bf16 bf16x2;
typedef __attribute__((ext_vector_type(4))) short s16x4;
typedef __attribute__((ext_vector_type(16))) float f32x16;
typedef __attribute__((ext_vector_type(4))) float f32x4;
typedef __attribute__((ext_vector_type(2))) float f32x2;
typedef __attribute__((ext_vector_type(4))) unsigned u32x4;
typedef __attribute__((ext_vector_type(2))) unsigned u32x2;
#define LAS __attribute__((address_space(3)))
#define GAS __attribute__((address_space(1)))
#define DEVI __device__ __forceinline__

#define PROF 0
#define STIME "s_memrealtime"
#define UOFF 0
#define W2OFF 0
#define W2STEP 1
#define W1STEP 1
#define HSTEP 1
#define FA_V 8
#define HFROM 5
#define PF1 8
#define PF0 0
#define PF0SLEEP 16
#define PF1N 1
#define PF2 7
#define TLBW 0
#define PPER 4
#define PSLEEP 8
#define PUB_L 1
#define PUB_P 2
#define PUB_U 1

constexpr size_t WS_EPOCH = 0;
constexpr size_t WS_TMO   = 128;
constexpr size_t WS_HFLAG = 4096;
constexpr size_t WS_RFLAG = 12288;
constexpr size_t WS_PFLAG = 16384;
constexpr size_t WS_UFLAG = 49152;
constexpr size_t WS_QFLAG = 81920;
constexpr size_t WS_FEND  = 114688;
constexpr size_t WS_G     = 114688;
constexpr size_t WS_Z     = 122880;
constexpr size_t WS_H     = 131072;
constexpr size_t WS_PART  = WS_H + 2097152;
constexpr size_t WS_U     = WS_PART + 32 * 32 * 1152;
constexpr size_t WS_PQ    = WS_U + 8 * 28 * 8192;
constexpr size_t WS_KW    = WS_PQ + 32 * 8 * 8192;
constexpr size_t WS_DBG   = WS_KW + 32 * 16384;
constexpr size_t WS_DBG2  = WS_DBG + 256 * 64 * 8;
constexpr size_t WS_END   = WS_DBG2 + 256 * 128 * 8;
constexpr int RING = 8;
constexpr int L_RW = 0, L_RA = RING * 8192, L_CW = 2 * RING * 8192;
constexpr int L_MIR = L_CW + 256;
constexpr int L_ZT = L_MIR + 1024 + 3 * 128;
constexpr int L_ZERO = L_ZT + 1024;
constexpr int L_PFS = L_ZERO + 64;
constexpr int L_RS = L_PFS + 256;
constexpr int L_BYTES = L_RS + 4 * 5120;
enum { W_TAG = 0, W_GATE = 4, W_CNT = 12 };
enum { G_H = 0, G_PQ = 1, G_R = 2, G_U = 3 };
enum { C_L = 0, C_PART = 1, C_U = 2, C_R = 3, C_RX = 4 };
constexpr unsigned SPIN_LDS = 1u << 19, SPIN_GLB = 1u << 15;

typedef volatile LAS unsigned* cw_t;

struct Params {
  const unsigned short *x, *wqkv, *wo, *w1, *b1, *w2, *b2, *gw;
  unsigned short* out;
  char* ws;
};

DEVI unsigned rfl(unsigned v) { return __builtin_amdgcn_readfirstlane(v); }
DEVI unsigned pk2(float a, float b) { f32x2 v = {a, b}; return __builtin_bit_cast(unsigned, __builtin_convertvector(v, bf16x2)); }
DEVI float bflo(unsigned u) { return __builtin_bit_cast(float, u << 16); }
DEVI float bfhi(unsigned u) { return __builtin_bit_cast(float, u & 0xffff0000u); }
DEVI f32x16 mfma32(bf16x8 a, bf16x8 b, f32x16 c) { return __builtin_amdgcn_mfma_f32_32x32x16_bf16(a, b, c, 0, 0, 0); }
DEVI bf16x8 lds_r(LAS char* p) { return *(LAS bf16x8*)p; }
DEVI bf16x8 lds_t(LAS char* p) {
  s16x4 a = __builtin_amdgcn_ds_read_tr16_b64_v4i16((LAS s16x4*)p);
  s16x4 b = __builtin_amdgcn_ds_read_tr16_b64_v4i16((LAS s16x4*)(p + 512));
  return __builtin_bit_cast(bf16x8, __builtin_shufflevector(a, b, 0, 1, 2, 3, 4, 5, 6, 7));
}
template <int CTRL> DEVI float dpp_add(float v) {
  return v + __builtin_bit_cast(float, __builtin_amdgcn_update_dpp(0, __builtin_bit_cast(int, v), CTRL, 0xf, 0xf, true));
}
DEVI float wave_sum(float v) {
  v = dpp_add<0xB1>(v); v = dpp_add<0x4E>(v); v = dpp_add<0x141>(v); v = dpp_add<0x140>(v);
  int iv = __builtin_bit_cast(int, v);
  float a = __builtin_bit_cast(float, __builtin_amdgcn_readlane(iv, 0)), b = __builtin_bit_cast(float, __builtin_amdgcn_readlane(iv, 16));
  float c = __builtin_bit_cast(float, __builtin_amdgcn_readlane(iv, 32)), d = __builtin_bit_cast(float, __builtin_amdgcn_readlane(iv, 48));
  return (a + b) + (c + d);
}
DEVI float xhalf(float v) {
  unsigned u = __builtin_bit_cast(unsigned, v);
  auto r = __builtin_amdgcn_permlane32_swap(u, u, false, false);
  return __builtin_bit_cast(float, (threadIdx.x & 32) ? r[0] : r[1]);
}
DEVI float phi(float t) {
  const float e = __builtin_amdgcn_exp2f(__builtin_fminf(t * 1.4426950408889634f, 0.f));
  return (__builtin_fmaxf(t, 0.f) + 1e-6f) + e;
}
DEVI float gelu_t(float u) {
  const float k1 = -1.5957691216057308f * 1.4426950408889634f, k3 = k1 * 0.044715f;
  const float e = __builtin_amdgcn_exp2f(u * __builtin_fmaf(u * u, k3, k1));
  return u * __builtin_amdgcn_rcpf(1.f + e);
}
#define VM0() asm volatile("s_waitcnt vmcnt(0)" ::: "memory")
#define VMN(n) asm volatile("s_waitcnt vmcnt(" #n ")" ::: "memory")
#define LGKM0() asm volatile("s_waitcnt lgkmcnt(0)" ::: "memory")
#define BAR() asm volatile("s_barrier" ::: "memory")
#define SCHED() __builtin_amdgcn_sched_barrier(0)
#define RLX_AGENT __ATOMIC_RELAXED, __HIP_MEMORY_SCOPE_AGENT

DEVI void stamp(const Params& p, int i) {
}

template <int SLP>
DEVI void poll_blocking(__amdgpu_buffer_rsrc_t rs, unsigned off, int n, bool wide, unsigned tag, int lane, char* ws) {
  const unsigned o = off + (unsigned)(lane < n ? lane : n - 1) * 128u;
  for (unsigned sp = 0;; ++sp) {
    bool ok;
    if (wide) { const u32x4 v = __builtin_amdgcn_raw_buffer_load_b128(rs, o, 0, 16); ok = v.x == tag && v.y == tag && v.z == tag && v.w == tag; }
    else ok = __builtin_amdgcn_raw_buffer_load_b32(rs, o, 0, 16) == tag;
    if (__all(ok)) break;
    if (sp > SPIN_GLB) { __hip_atomic_store((GAS unsigned*)(ws + WS_TMO), 7u, RLX_AGENT); break; }
    __builtin_amdgcn_s_sleep(SLP);
  }
  asm volatile("" ::: "memory");
}

template <bool SC1>
DEVI void dma8(unsigned m0v, const char* b0, const char* b1, unsigned v0, unsigned v1, unsigned v2, unsigned v3) {
  constexpr bool USE_SC1 = SC1;
  if (USE_SC1)
    asm volatile("s_mov_b32 m0, %0\n\ts_nop 0\n\t"
                 "global_load_lds_dwordx4 %1, %5 sc1\n\t"
                 "global_load_lds_dwordx4 %2, %5 offset:1024 sc1\n\t"
                 "global_load_lds_dwordx4 %3, %5 offset:2048 sc1\n\t"
                 "global_load_lds_dwordx4 %4, %5 offset:3072 sc1\n\t"
                 "s_add_u32 m0, m0, 0x1000\n\ts_nop 0\n\t"
                 "global_load_lds_dwordx4 %1, %6 sc1\n\t"
                 "global_load_lds_dwordx4 %2, %6 offset:1024 sc1\n\t"
                 "global_load_lds_dwordx4 %3, %6 offset:2048 sc1\n\t"
                 "global_load_lds_dwordx4 %4, %6 offset:3072 sc1"
                 :: "s"(m0v), "v"(v0), "v"(v1), "v"(v2), "v"(v3), "s"(b0), "s"(b1) : "memory", "scc");
  else
    asm volatile("s_mov_b32 m0, %0\n\ts_nop 0\n\t"
                 "global_load_lds_dwordx4 %1, %5\n\t"
                 "global_load_lds_dwordx4 %2, %5 offset:1024\n\t"
                 "global_load_lds_dwordx4 %3, %5 offset:2048\n\t"
                 "global_load_lds_dwordx4 %4, %5 offset:3072\n\t"
                 "s_add_u32 m0, m0, 0x1000\n\ts_nop 0\n\t"
                 "global_load_lds_dwordx4 %1, %6\n\t"
                 "global_load_lds_dwordx4 %2, %6 offset:1024\n\t"
                 "global_load_lds_dwordx4 %3, %6 offset:2048\n\t"
                 "global_load_lds_dwordx4 %4, %6 offset:3072"
                 :: "s"(m0v), "v"(v0), "v"(v1), "v"(v2), "v"(v3), "s"(b0), "s"(b1) : "memory", "scc");
}
DEVI void wait_vm_tiles(int n) {
  if (n >= 4) { if (n >= 6) { if (n >= 7) VMN(56); else VMN(48); } else { if (n == 5) VMN(40); else VMN(32); } }
  else { if (n >= 2) { if (n == 3) VMN(24); else VMN(16); } else { if (n == 1) VMN(8); else VM0(); } }
}
DEVI int used(int i) { return i < 8 ? 2 * i + 2 : i + 9; }
struct Loader {
  cw_t cw; char* ws; __amdgpu_buffer_rsrc_t rsF; unsigned ldsb; int I, i, lane;
  template <bool SC1> DEVI void issue(const char* base, long half, unsigned v0, unsigned v1, unsigned v2, unsigned v3) {
    dma8<SC1>(ldsb + (unsigned)(I & (RING - 1)) * 8192u, base, base + half, v0, v1, v2, v3); ++I;
  }
  DEVI void ztable() {
    asm volatile("s_mov_b32 m0, %0\n\ts_nop 0\n\tglobal_load_lds_dwordx4 %1, %2 sc1" :: "s"((unsigned)L_ZT), "v"((unsigned)lane * 16u), "s"(ws + WS_Z) : "memory");
  }
  template <bool SC1, bool ZT>
  DEVI void stream(int end, const char* base, long step, long half, unsigned v0, unsigned v1, unsigned v2, unsigned v3, int gate, unsigned foff, int fn) {
    if (gate >= 0) {
      for (;;) {
        const unsigned gw = *(LAS unsigned*)((LAS char*)cw + (W_GATE + gate) * 4);
        const int d = I - used(i);
        if (d < 0) {
          if (rfl(gw) == 0u) {
            poll_blocking<PSLEEP>(rsF, foff, fn, gate == G_H, cw[W_TAG], lane, ws); if (lane == 0) cw[W_GATE + gate] = 1u;
          }
          break;
        }
        wait_vm_tiles(d); BAR(); ++i;
        if (rfl(gw) != 0u) break;
      }
      if (ZT) ztable();
      const int lim = used(i - 2) + RING;
      while (I < lim && I < end) { issue<SC1>(base, half, v0, v1, v2, v3); base += step; }
    }
    while (I < end) {
      if (i >= 9 && I == i + 7 + RING) {
        const int n0 = end - I;
#pragma unroll 1
        for (int n = n0; n > 0; --n) { VMN(48); BAR(); issue<SC1>(base, half, v0, v1, v2, v3); base += step; }
        i += n0;
        break;
      }
      wait_vm_tiles(I - used(i)); BAR();
      const int lim = used(i - 1) + RING; ++i;
      while (I < lim && I < end) { issue<SC1>(base, half, v0, v1, v2, v3); base += step; }
    }
  }
  DEVI void tail(int ntot) { while (i < ntot) { wait_vm_tiles(I - used(i)); BAR(); ++i; } }
};

struct Frag { bf16x8 a[4], b[4]; };
DEVI void mf0(f32x16& d, bf16x8 a, bf16x8 b) { asm volatile("v_mfma_f32_32x32x16_bf16 %0, %1, %2, 0" : "=&v"(d) : "v"(a), "v"(b)); }
DEVI void mfa(f32x16& d, bf16x8 a, bf16x8 b) { asm volatile("v_mfma_f32_32x32x16_bf16 %0, %1, %2, %0" : "+v"(d) : "v"(a), "v"(b)); }
enum { K_T = 0, K_H = 2 };
template <int KIND> DEVI bf16x8 ld_frag1(LAS char* slot, unsigned off, int t) {
  if (KIND == K_T) return lds_t(slot + off + t * 2048);
  else return lds_r(slot + (off ^ ((t & 1) << 5)) + (t >> 1) * 4096);
}
template <int KA, int KB>
DEVI void load_frag(Frag& f, LAS char* sa, LAS char* sb, unsigned offA, unsigned offB) {
#pragma unroll
  for (int t = 0; t < 4; ++t) { f.a[t] = ld_frag1<KA>(sa, offA, t); f.b[t] = ld_frag1<KB>(sb, offB, t); }
}
DEVI void mma4(f32x16& acc, const Frag& f) {
#pragma unroll
  for (int t = 0; t < 4; ++t) acc = mfma32(f.a[t], f.b[t], acc);
}
DEVI void interleave() {
  __builtin_amdgcn_sched_group_barrier(0x100, 6, 0); __builtin_amdgcn_sched_group_barrier(0x8, 1, 0);
  __builtin_amdgcn_sched_group_barrier(0x100, 6, 0); __builtin_amdgcn_sched_group_barrier(0x8, 1, 0);
  __builtin_amdgcn_sched_group_barrier(0x100, 4, 0); __builtin_amdgcn_sched_group_barrier(0x8, 2, 0);
}
template <int NT, bool A_FROM_W, int KA, int KB, class Hook>
DEVI void kloop(f32x16& acc, LAS char* lds, int& tc, unsigned offA, unsigned offB, Hook hook) {
  Frag f0, f1;
  auto ld = [&](Frag& f) {
    const unsigned sw = (unsigned)(tc & (RING - 1)) * 8192u + L_RW, sa = sw + (L_RA - L_RW);
    const unsigned s1 = A_FROM_W ? sw : sa, s2 = A_FROM_W ? sa : sw;
    load_frag<KA, KB>(f, lds + s1, lds + s2, offA, offB);
    ++tc;
  };
  BAR(); SCHED();
  ld(f0);
  SCHED(); LGKM0();
  hook(0);
#pragma unroll 1
  for (int i = 1; i < NT - 1; i += 2) {
    BAR(); SCHED();
    ld(f1); mma4(acc, f0); interleave();
    SCHED(); LGKM0();
    hook(i);
    BAR(); SCHED();
    ld(f0); mma4(acc, f1); interleave();
    SCHED(); LGKM0();
    hook(i + 1);
  }
  BAR(); SCHED();
  ld(f1); mma4(acc, f0); interleave();
  asm volatile("" : "+v"(acc));
  SCHED(); LGKM0();
  hook(NT - 1);
  mma4(acc, f1);
  asm volatile("" : "+v"(acc));
}
DEVI void store_quadrant(const float (&v)[16], __amdgpu_buffer_rsrc_t rs, unsigned tbase, int qa, int qb, int lane) {
  unsigned x[2][4];
#pragma unroll
  for (int kp = 0; kp < 2; ++kp) {
    const int g = 2 * kp;
    unsigned a0 = pk2(v[4 * g + 0], v[4 * g + 1]), a1 = pk2(v[4 * g + 2], v[4 * g + 3]);
    unsigned b0 = pk2(v[4 * g + 4], v[4 * g + 5]), b1 = pk2(v[4 * g + 6], v[4 * g + 7]);
    auto r0 = __builtin_amdgcn_permlane32_swap(a0, b0, false, false);
    auto r1 = __builtin_amdgcn_permlane32_swap(a1, b1, false, false);
    x[kp][0] = r0[0]; x[kp][1] = r1[0]; x[kp][2] = r0[1]; x[kp][3] = r1[1];
  }
  u32x4 y0, y1;
#pragma unroll
  for (int i = 0; i < 4; ++i) { auto r = __builtin_amdgcn_permlane16_swap(x[0][i], x[1][i], false, false); y0[i] = r[0]; y1[i] = r[1]; }
  const unsigned row = lane & 15, c = 2u * ((lane >> 4) & 1) + (lane >> 5);
  const unsigned o = tbase + qa * 4096u + (32u * qb + row) * 64u + ((c ^ (row >> 2)) << 4);
  __builtin_amdgcn_raw_buffer_store_b128(y0, rs, o, 0, 16);
  __builtin_amdgcn_raw_buffer_store_b128(y1, rs, o + 1024u, 0, 16);
}
DEVI void publish(const Params& p, cw_t cw, int cnt, GAS unsigned* flag, GAS unsigned* flag2, int lane) {
  VM0();
  if (lane == 0) {
    unsigned old = __hip_atomic_fetch_add((LAS unsigned*)(cw + W_CNT + cnt), 1u, __ATOMIC_RELAXED, __HIP_MEMORY_SCOPE_WORKGROUP);
    if (old == 3u) { const unsigned tg = cw[W_TAG]; __hip_atomic_store(flag, tg, RLX_AGENT); if (flag2) __hip_atomic_store(flag2, tg, RLX_AGENT);
    }
  }
}

__global__ void __launch_bounds__(448 + 64 * PROF, 2) fused_k(Params p) {
  extern __shared__ __attribute__((aligned(16))) char shm[];
  unsigned long long kstart; asm volatile("s_getpc_b64 %0" : "=s"(kstart));
  LAS char* lds = (LAS char*)shm;
  cw_t cw = (cw_t)(lds + L_CW);
  const int tid = threadIdx.x, lane = tid & 63;
  const int wid = __builtin_amdgcn_readfirstlane(tid >> 6);
  const int b = blockIdx.x, xcd = b & 7, idx = b >> 3;
  const int st = (xcd >> 1) * 8 + (idx >> 2), jj_ = (xcd & 1) * 4 + (idx & 3);
  const bool isR = idx >= 28;
  const int ft = (xcd >> 1) * 7 + (idx >> 2);
  const int rh = xcd * 4 + (idx & 3);
  const int NG1 = isR ? 0 : 32;
  const int NTOT = 8 + NG1 + 8 + 28;
  char* ws = p.ws;

  if (wid == 4 || wid == 5) {
    const int isA = wid - 4;
    const int rr = lane >> 3, cc = lane & 7;
    Loader L; L.cw = cw; L.ws = ws; L.lane = lane; L.ldsb = (unsigned)(isA ? L_RA : L_RW); L.I = 0;
    L.rsF = __builtin_amdgcn_make_buffer_rsrc(ws, 0, (int)WS_FEND, 0x00020000);
    __builtin_amdgcn_s_setprio(3);
    const unsigned cT = (unsigned)(cc ^ (((rr >> 1) & 1) << 2));
    const unsigned cR0 = (unsigned)(cc ^ (rr >> 1)), cR1 = cR0 ^ 4u;
    auto vT = [&](int j, unsigned sb, unsigned c) { return (unsigned)(8 * j + rr) * sb + c * 16u - j * 1024u; };
    {
      const char* b0; const char* b1; long st0, h0; unsigned p0, p1, p2, p3, x1;
      if (!isA) {
        b0 = (const char*)p.wqkv + (512 + jj_ * 64) * 2; b1 = b0 + 1024; st0 = 64 * 3072; h0 = 32 * 3072;
        p0 = vT(0, 3072, cT); p1 = vT(1, 3072, cT); p2 = vT(2, 3072, cT); p3 = vT(3, 3072, cT); x1 = 2;
      } else {
        b0 = (const char*)p.x + (size_t)(st * 64) * 1024; st0 = 128; h0 = 32 * 1024;
        b1 = (const char*)p.wqkv + jj_ * 128;
        p0 = vT(0, 1024, cR0); p1 = vT(1, 1024, cR1); p2 = vT(2, 1024, cR0); p3 = vT(3, 1024, cR1); x1 = 0;
      }
      const unsigned q0 = vT(0, 3072, cT ^ x1), q1 = vT(1, 3072, cT ^ x1), q2 = vT(2, 3072, cT ^ x1), q3 = vT(3, 3072, cT ^ x1);
      asm volatile("" :: "v"(p0), "v"(p1), "v"(p2), "v"(p3), "v"(q0), "v"(q1), "v"(q2), "v"(q3), "s"(b0), "s"(b1));
      BAR();
      for (int k = 0; k < 4; ++k) { L.issue<false>(b0, h0, p0, p1, p2, p3); L.issue<false>(b1, 32 * 3072, q0, q1, q2, q3); b0 += st0; b1 += 64 * 3072; }
      VMN(48); BAR();
#pragma unroll 1
      for (int k = 1; k <= 4; ++k) {
        VMN(32); BAR();
        L.issue<false>(b0, h0, p0, p1, p2, p3); L.issue<false>(b1, 32 * 3072, q0, q1, q2, q3); b0 += st0; b1 += 64 * 3072;
      }
      L.i = 5;
    }
    stamp(p, isA ? 48 : 32);
    const unsigned l0 = (unsigned)lane * 16u;
    if (!isA) {
      if (!isR) L.stream<false, false>(48, (const char*)p.w1 + ft * 128, W1STEP * 64L * 3584, 32L * 3584, vT(0, 3584, cT), vT(1, 3584, cT), vT(2, 3584, cT), vT(3, 3584, cT), -1, 0, 0);
      stamp(p, 33);
      L.stream<true, true>(16 + NG1 + 8, ws + WS_KW + (size_t)(jj_ * 8) * 8192, 8192, 4096, l0, l0, l0, l0, G_R, (unsigned)WS_RFLAG, 32);
      stamp(p, 34);
      L.stream<false, false>(16 + NG1 + 8 + 28, (const char*)p.w2 + W2OFF + st * 128, W2STEP * 64L * 4096, 32L * 4096, vT(0, 4096, cT), vT(1, 4096, cT), vT(2, 4096, cT), vT(3, 4096, cT), -1, 0, 0);
      stamp(p, 35);
    } else {
      if (!isR) L.stream<true, false>(48, ws + WS_H + (size_t)jj_ * 8192, HSTEP * 8L * 8192, 4096, l0, l0, l0, l0, G_H, (unsigned)WS_HFLAG, 64);
      stamp(p, 49);
      L.stream<true, false>(16 + NG1 + 8, ws + WS_PQ + (size_t)(st * 8) * 8192, 8192, 4096, l0, l0, l0, l0, G_PQ, (unsigned)WS_QFLAG + st * 1024, 8);
      stamp(p, 50);
      L.stream<true, false>(16 + NG1 + 8 + 28, ws + WS_U + UOFF + (size_t)(jj_ * 28) * 8192, 8192, 4096, l0, l0, l0, l0, G_U, (unsigned)WS_UFLAG + jj_ * 4096, 28);
      stamp(p, 51);
    }
    L.tail(NTOT);
    VM0();
    stamp(p, isA ? 52 : 36);
    return;
  }

  if (wid == 6) {
    {
      u32x4 z = {0u, 0u, 0u, 0u};
      *(LAS u32x4*)(lds + L_CW + lane * 16) = z;
      if (lane < 40) *(LAS u32x4*)(lds + L_CW + 1024 + lane * 16) = z;
      if (lane == 63) *(LAS u32x4*)(lds + L_ZERO) = z;
    }
    unsigned tag;
    asm volatile("global_load_dword %0, %1, %2 sc1" : "=v"(tag) : "v"(0u), "s"(ws + WS_EPOCH) : "memory");
    const char* pf1 = (const char*)p.w1 + (size_t)(idx * 64) * 3584 + (xcd >> 1) * 7 * 128;
    const char* pf2 = (const char*)p.w2 + (size_t)(idx * 56) * 4096 + (xcd >> 1) * 1024;
    const unsigned pv1 = (unsigned)lane * 3584u, pv2 = (unsigned)(lane >> 3) * 4096u + (unsigned)(lane & 7) * 128u;
    {
      const char* c0 = (const char*)(kstart & ~127ull);
      const unsigned o = (unsigned)idx * 8192u + (unsigned)lane * 128u;
      asm volatile("s_mov_b32 m0, %0\n\ts_nop 0\n\tglobal_load_lds_dword %1, %2" :: "s"((unsigned)L_PFS), "v"(o < CODEPF ? o : 0u), "s"(c0) : "memory");
    }
    BAR();
    asm volatile("s_waitcnt vmcnt(%1)" : "+v"(tag) : "i"(PF0 + (TLBW > 0 ? 7 : 0) + (CODEPF > 0 ? 1 : 0)) : "memory");
    tag = rfl(tag) + 1u; if (tag == 0u) tag = 1u;
    if (lane == 0) cw[W_TAG] = tag;
    LGKM0();
    int left = NTOT;
    auto prefetch = [&](int i_) {
      const int i = (int)rfl((unsigned)i_);
      if (i >= PF1 && (i - PF1) * PF1N < 7 - PF0) {
        for (int k = PF0 + (i - PF1) * PF1N; k < PF0 + (i - PF1 + 1) * PF1N && k < 7; ++k)
          asm volatile("s_mov_b32 m0, %0\n\ts_nop 0\n\tglobal_load_lds_dword %1, %2" :: "s"((unsigned)L_PFS), "v"(pv1), "s"(pf1 + k * 128) : "memory");
      }
      const int i2 = i - (NG1 + PF2);
      if (i2 >= 0 && i2 < 7) asm volatile("s_mov_b32 m0, %0\n\ts_nop 0\n\tglobal_load_lds_dword %1, %2" :: "s"((unsigned)L_PFS), "v"(pv2), "s"(pf2 + i2 * 8 * 4096) : "memory");
    };
#define PBAR() do { BAR(); prefetch(NTOT - left); } while (0)
    auto gate_loop = [&](int g, const char* fb, unsigned moff, int n, auto WIDE, int from, int pmask) {
#pragma unroll 1
      while (left > NTOT - from) { PBAR(); --left; }
      constexpr bool wide = decltype(WIDE)::value;
      const int lc = lane < n ? lane : n - 1;
      const unsigned voff = (unsigned)lc * 128u;
      const unsigned roff = moff + (unsigned)lc * (wide ? 16u : 4u);
      u32x4 v = {0u, 0u, 0u, 0u}; unsigned gw = 0u;
      while (left > 0) {
        PBAR(); --left;
        if (__all((v.x == tag && v.y == tag && v.z == tag && v.w == tag) || gw != 0u)) {
          if (lane == 0) cw[W_GATE + g] = 1u;
          stamp(p, 24 + g);
          break;
        }
        if ((left & pmask) == 0 && lane < n) {
          if (wide) asm volatile("s_mov_b32 m0, %0\n\ts_nop 0\n\tglobal_load_lds_dwordx4 %1, %2 sc1" :: "s"(moff), "v"(voff), "s"(fb) : "memory");
          else asm volatile("s_mov_b32 m0, %0\n\ts_nop 0\n\tglobal_load_lds_dword %1, %2 sc1" :: "s"(moff), "v"(voff), "s"(fb) : "memory");
        }
        if (wide) v = *(LAS u32x4*)(lds + roff);
        else { const unsigned x = *(LAS unsigned*)(lds + roff); v = (u32x4){x, x, x, x}; }
        gw = *(LAS unsigned*)(lds + L_CW + (W_GATE + g) * 4);
      }
    };
    gate_loop(G_H, ws + WS_HFLAG, L_MIR, 64, std::true_type{}, HFROM, 0);
    if (b == 0 && lane == 0) __hip_atomic_store((GAS unsigned*)(ws + WS_EPOCH), tag, RLX_AGENT);
    gate_loop(G_PQ, ws + WS_QFLAG + st * 1024, L_MIR + 1024, 8, std::false_type{}, 8 + NG1 - 12, PPER - 1);
    gate_loop(G_R, ws + WS_RFLAG, L_MIR + 1024 + 128, 32, std::false_type{}, 8 + NG1 - 10, PPER - 1);
    gate_loop(G_U, ws + WS_UFLAG + jj_ * 4096, L_MIR + 1024 + 256, 28, std::false_type{}, 8 + NG1 + 1, 0);
#pragma unroll 1
    for (; left > 0; --left) PBAR();
    VM0();
    return;
  }

  const int w = wid, qa = w & 1, qb = w >> 1, h = lane >> 5;
  const __amdgpu_buffer_rsrc_t rsW = __builtin_amdgcn_make_buffer_rsrc(ws, 0, (int)WS_END, 0x00020000);
  stamp(p, w == 0 ? 0 : 63);
  {
    const int rl = jj_ * 8 + 2 * w, r0 = st * 64 + rl;
    u32x4 xv[2];
    xv[0] = *(const GAS u32x4*)((const GAS char*)p.x + (size_t)r0 * 1024 + lane * 16);
    xv[1] = *(const GAS u32x4*)((const GAS char*)p.x + (size_t)(r0 + 1) * 1024 + lane * 16);
    const u32x4 gv = *(const GAS u32x4*)((const GAS char*)p.gw + lane * 16);
    BAR();
    u32x4 o[2]; float gt[2];
#pragma unroll
    for (int r = 0; r < 2; ++r) {
      float x[8], s = 0.f, gd = 0.f;
#pragma unroll
      for (int i = 0; i < 4; ++i) { x[2 * i] = bflo(xv[r][i]); x[2 * i + 1] = bfhi(xv[r][i]); gd += x[2 * i] * bflo(gv[i]) + x[2 * i + 1] * bfhi(gv[i]); }
#pragma unroll
      for (int i = 0; i < 8; ++i) s += x[i];
      const float m = wave_sum(s) * (1.f / 512.f);
      float q = 0.f;
#pragma unroll
      for (int i = 0; i < 8; ++i) { x[i] -= m; q += x[i] * x[i]; }
      const float rs = __builtin_amdgcn_rsqf(wave_sum(q) * (1.f / 512.f) + 1e-5f);
      gt[r] = __builtin_amdgcn_rcpf(1.f + __expf(-wave_sum(gd)));
      o[r] = (u32x4){pk2(x[0] * rs, x[1] * rs), pk2(x[2] * rs, x[3] * rs), pk2(x[4] * rs, x[5] * rs), pk2(x[6] * rs, x[7] * rs)};
    }
    asm volatile("" : "+v"(o[0]), "+v"(o[1]), "+v"(gt[0]), "+v"(gt[1]));
#pragma unroll
    for (int r = 0; r < 2; ++r) {
      const int row = rl + r;
      const unsigned cs = (unsigned)(lane & 7) ^ (unsigned)(((row >> 1) & 1) << 2);
      __builtin_amdgcn_raw_buffer_store_b128(o[r], rsW, (unsigned)WS_H + (unsigned)((st * 8 + (lane >> 3)) * 8192 + row * 128) + cs * 16u, 0, 16);
    }
    if (lane == 0) { f32x2 gg = {gt[0], gt[1]}; __builtin_amdgcn_raw_buffer_store_b64(__builtin_bit_cast(u32x2, gg), rsW, (unsigned)(WS_G + r0 * 4), 0, 16); }
  }
  stamp(p, w == 0 ? 1 : 63);

  const int blk = (lane >> 4) & 1, qq = (lane & 15) >> 2, pp = lane & 3;
  const unsigned row0 = 8 * h + qq, swT = ((qq >> 1) & 1) << 2;
  auto offT = [&](int q) { return row0 * 128u + (((unsigned)(4 * q + 2 * blk + (pp >> 1)) ^ swT) << 4) + 8u * (pp & 1); };
  auto offR = [&](int q) { return (unsigned)(32 * q + (lane & 31)) * 128u + (((unsigned)h ^ ((lane >> 1) & 7u)) << 4); };
  auto offH = [&](int q) { return (unsigned)(32 * q + (lane & 31)) * 64u + (((unsigned)h ^ ((lane >> 2) & 3u)) << 4); };
  int tc = 0;

  {
    f32x16 a0 = {}, a1 = {}, aq = {};
    const unsigned oB = blk * 8192u + row0 * 128u + (((unsigned)(2 * w + (pp >> 1)) ^ swT ^ (unsigned)(blk << 1)) << 4) + 8u * (pp & 1);
    const unsigned oA0 = offR(qb), oA1 = offR(1 - qb), oQ = 8192u + offT(qa);
    struct F0 { bf16x8 a0[4], a1[4], b[4], q[4]; };
    F0 f0, f1;
    auto ld = [&](F0& f) {
      const unsigned sl = (unsigned)(tc & (RING - 1)) * 8192u;
      LAS char* sw = lds + sl + L_RW + oB;
      LAS char* sa = lds + sl + L_RA;
#pragma unroll
      for (int t = 0; t < 4; ++t) { f.b[t] = lds_t(sw + t * 2048); f.a0[t] = lds_r(sa + (oA0 ^ (t << 5))); f.a1[t] = lds_r(sa + (oA1 ^ (t << 5))); f.q[t] = lds_t(sa + oQ + t * 2048); }
      tc += 2;
    };
    auto mm = [&](const F0& f) {
#pragma unroll
      for (int t = 0; t < 4; ++t) { a0 = mfma32(f.a0[t], f.b[t], a0); a1 = mfma32(f.a1[t], f.b[t], a1); aq = mfma32(f.q[t], f.a0[t], aq); }
#pragma unroll
      for (int t = 0; t < 8; ++t) { __builtin_amdgcn_sched_group_barrier(0x100, 3, 0); __builtin_amdgcn_sched_group_barrier(0x8, 1, 0); }
      __builtin_amdgcn_sched_group_barrier(0x8, 4, 0);
    };
    BAR(); SCHED(); ld(f0); SCHED(); LGKM0();
#pragma unroll 1
    for (int i = 1; i < 7; i += 2) {
      BAR(); SCHED(); ld(f1); mm(f0); SCHED(); LGKM0();
      if (i == 1) publish(p, cw, C_L, (GAS unsigned*)(ws + WS_HFLAG) + (b >> 2) * 32 + (b & 3), nullptr, lane);
      BAR(); SCHED(); ld(f0); mm(f1); SCHED(); LGKM0();
    }
    BAR(); SCHED(); ld(f1); mm(f0); asm volatile("" : "+v"(a0), "+v"(a1), "+v"(aq)); SCHED(); LGKM0();
    mm(f1);
    asm volatile("" : "+v"(a0), "+v"(a1), "+v"(aq));
    stamp(p, w == 0 ? 2 : 63);
    {
      float v[16];
#pragma unroll
      for (int i = 0; i < 16; ++i) v[i] = phi(aq[i]);
      store_quadrant(v, rsW, (unsigned)(WS_PQ + (size_t)(st * 8 + jj_) * 8192), qa, qb, lane);
    }
    const bool isK = blk == 0;
    f32x16 gr = {};
#pragma unroll
    for (int q = 0; q < 2; ++q) {
#pragma unroll
      for (int ss = 0; ss < 2; ++ss) {
        u32x4 fa, fbv;
#pragma unroll
        for (int i = 0; i < 4; ++i) {
          float v0 = q ? a1[8 * ss + 2 * i] : a0[8 * ss + 2 * i], v1 = q ? a1[8 * ss + 2 * i + 1] : a0[8 * ss + 2 * i + 1];
          const float p0 = phi(v0), p1 = phi(v1);
          v0 = isK ? p0 : v0; v1 = isK ? p1 : v1;
          fa[i] = pk2(v0, v1);
          fbv[i] = isK ? 0x3F803F80u : fa[i];
        }
        gr = mfma32(__builtin_bit_cast(bf16x8, fa), __builtin_bit_cast(bf16x8, fbv), gr);
      }
    }
    const unsigned rec = (unsigned)(WS_PART + ((size_t)(jj_ * 4 + w) * 32 + st) * 1152);
    if (blk == 1 || (lane & 31) == 0) {
#pragma unroll
      for (int g = 0; g < 2; ++g) {
        f32x4 v = {gr[4 * g], gr[4 * g + 1], gr[4 * g + 2], gr[4 * g + 3]};
        const unsigned o = blk == 1 ? (unsigned)((g * 2 + h) * 256 + (lane & 15) * 16) : (unsigned)(1024 + (g * 2 + h) * 16);
        __builtin_amdgcn_raw_buffer_store_b128(__builtin_bit_cast(u32x4, v), rsW, rec + o, 0, 16);
      }
    }
    stamp(p, w == 0 ? 3 : 63);
  }
  GAS unsigned* const pflag = (GAS unsigned*)(ws + WS_PFLAG) + (jj_ * 32 + st) * 32;
  GAS unsigned* const qflag = (GAS unsigned*)(ws + WS_QFLAG) + (st * 8 + jj_) * 32;

  if (!isR) {
    if (PUB_P < 0) publish(p, cw, C_PART, pflag, qflag, lane);
    f32x16 acc;
#pragma unroll
    for (int g = 0; g < 4; ++g) {
      const u32x2 bb = *(const GAS u32x2*)((const GAS char*)p.b1 + (ft * 64 + 32 * qa + 8 * g + 4 * h) * 2);
      acc[4 * g + 0] = bflo(bb.x); acc[4 * g + 1] = bfhi(bb.x); acc[4 * g + 2] = bflo(bb.y); acc[4 * g + 3] = bfhi(bb.y);
    }
    kloop<32, true, K_T, K_T>(acc, lds, tc, offT(qa), offT(qb), [&](int i) { if (i == PUB_P) publish(p, cw, C_PART, pflag, qflag, lane); });
    stamp(p, w == 0 ? 4 : 63);
    float v[16];
#pragma unroll
    for (int k = 0; k < 16; ++k) v[k] = gelu_t(acc[k]);
    store_quadrant(v, rsW, (unsigned)(WS_U + (size_t)(jj_ * 28 + ft) * 8192), qa, qb, lane);
    stamp(p, w == 0 ? 5 : 63);
  } else {
    publish(p, cw, C_PART, pflag, qflag, lane);
    LAS char* sc = lds + L_RS + w * 5120;
#pragma unroll
    for (int i = 0; i < 4; ++i) {
      const int e = i * 4 + (lane >> 4);
      const u32x4 wv = *(const GAS u32x4*)((const GAS char*)p.wo + (size_t)(rh * 16 + e) * 1024 + w * 256 + (lane & 15) * 16);
      *(LAS u32x4*)(sc + e * 256 + (lane & 15) * 16) = wv;
    }
    stamp(p, w == 0 ? 20 : 63);
    poll_blocking<1>(rsW, (unsigned)WS_PFLAG + (rh >> 2) * 4096, 32, false, cw[W_TAG], lane, ws);
    stamp(p, w == 0 ? 21 : 63);
    const unsigned pb = (unsigned)(WS_PART + (size_t)rh * 32 * 1152);
    f32x4 kv, zz;
    {
      u32x4 pv[8];
#pragma unroll
      for (int t = 0; t < 8; ++t) pv[t] = __builtin_amdgcn_raw_buffer_load_b128(rsW, pb + (w * 8 + t) * 1152 + lane * 16, 0, 16);
      const u32x4 z0 = __builtin_amdgcn_raw_buffer_load_b128(rsW, pb + (w * 8 + ((lane >> 2) & 7)) * 1152 + 1024 + (lane & 3) * 16, 0, 16);
      kv = __builtin_bit_cast(f32x4, pv[0]);
#pragma unroll
      for (int t = 1; t < 8; ++t) kv += __builtin_bit_cast(f32x4, pv[t]);
      zz = __builtin_bit_cast(f32x4, z0);
#pragma unroll
      for (int k = 0; k < 4; ++k) { float v = zz[k]; v += __shfl_xor(v, 4); v += __shfl_xor(v, 8); v += __shfl_xor(v, 16); zz[k] = v; }
      LAS f32x4* ex = (LAS f32x4*)(lds + L_RW + 32768);
      ex[w * 68 + lane] = kv;
      if (lane < 4) ex[w * 68 + 64 + lane] = zz;
      LGKM0();
      if (lane == 0) (void)__hip_atomic_fetch_add((LAS unsigned*)(cw + W_CNT + C_RX), 1u, __ATOMIC_RELAXED, __HIP_MEMORY_SCOPE_WORKGROUP);
      for (unsigned sp = 0; cw[W_CNT + C_RX] < 4u && sp < SPIN_LDS; ++sp) __builtin_amdgcn_s_sleep(1);
      asm volatile("" ::: "memory");
      kv = (ex[lane] + ex[68 + lane]) + (ex[136 + lane] + ex[204 + lane]);
      zz = (ex[64 + (lane & 3)] + ex[132 + (lane & 3)]) + (ex[200 + (lane & 3)] + ex[268 + (lane & 3)]);
    }
    asm volatile("" : "+v"(kv), "+v"(zz));
    stamp(p, w == 0 ? 22 : 63);
    {
      const int e = lane & 15, d0 = (lane >> 5) * 8 + ((lane >> 4) & 1) * 4;
#pragma unroll
      for (int i = 0; i < 4; ++i) *(LAS unsigned short*)(sc + 4096 + (d0 + i) * 32 + e * 2) = (unsigned short)(pk2(kv[i], 0.f) & 0xffffu);
    }
    if (w == 0 && lane < 4) {
      u32x2 zv = {pk2(zz[0], zz[1]), pk2(zz[2], zz[3])};
      __builtin_amdgcn_raw_buffer_store_b64(zv, rsW, (unsigned)(WS_Z + rh * 32 + lane * 8), 0, 16);
    }
    LGKM0();
    u32x4 af = *(LAS u32x4*)(sc + 4096 + (lane & 15) * 32 + ((lane >> 4) & 1) * 16);
    if (lane >= 32) af = (u32x4){0u, 0u, 0u, 0u};
    const int gq = (lane >> 4) & 1, q4 = (lane & 15) >> 2, p4 = lane & 3;
#pragma unroll
    for (int c = 0; c < 8; ++c) {
      LAS char* a = sc + (8 * gq + q4) * 256 + c * 32 + p4 * 8;
      s16x4 t0 = __builtin_amdgcn_ds_read_tr16_b64_v4i16((LAS s16x4*)a);
      s16x4 t1 = __builtin_amdgcn_ds_read_tr16_b64_v4i16((LAS s16x4*)(a + 4 * 256));
      bf16x8 bfm = __builtin_bit_cast(bf16x8, __builtin_shufflevector(t0, t1, 0, 1, 2, 3, 4, 5, 6, 7));
      f32x4 o = __builtin_amdgcn_mfma_f32_16x16x32_bf16(__builtin_bit_cast(bf16x8, af), bfm, (f32x4){0.f, 0.f, 0.f, 0.f}, 0, 0, 0);
      u32x2 ov = {pk2(o[0], o[1]), pk2(o[2], o[3])};
      const unsigned n = w * 128 + c * 16 + (lane & 15), nr = n & 63u, q = lane >> 4;
      __builtin_amdgcn_raw_buffer_store_b64(ov, rsW, (unsigned)WS_KW + ((n >> 6) * 8 + (rh >> 2)) * 8192u + ((rh >> 1) & 1) * 4096u + nr * 64u + (((2u * (rh & 1) + (q >> 1)) ^ ((nr >> 2) & 3u)) << 4) + (q & 1) * 8u, 0, 16);
    }
    publish(p, cw, C_R, (GAS unsigned*)(ws + WS_RFLAG) + rh * 32, nullptr, lane);
    stamp(p, w == 0 ? 5 : 63);
  }

  {
    f32x16 aa = {};
    {
      const unsigned oA = offH(qa), oB = offH(qb);
      const int zm = lane & 31;
      unsigned zo[4];
#pragma unroll
      for (int t = 0; t < 4; ++t) zo[t] = (zm < 8 && (zm & 3) == t) ? (unsigned)(L_ZT + t * 32 + h * 16) : (unsigned)L_ZERO;
      Frag f0, f1; bf16x8 z0[4], z1[4];
      f32x16 nm[4]; float inv[4];
      auto l1 = [&](Frag& fn, bf16x8 (&zn)[4], int hg, int t) {
        LAS char* const sw = lds + (unsigned)(tc & (RING - 1)) * 8192u + L_RW;
        fn.a[t] = ld_frag1<K_H>(sw, oA, t); fn.b[t] = ld_frag1<K_H>(sw + (L_RA - L_RW), oB, t);
        const unsigned zs = (zm < 8 && (zm & 3) == t) ? (unsigned)hg * 128u : 0u; zn[t] = lds_r(lds + zo[t] + zs);
      };
      auto step = [&](const Frag& f, const bf16x8 (&z)[4], auto FIRST, auto LOAD, Frag& fn, bf16x8 (&zn)[4], int hgn) {
        constexpr bool first = decltype(FIRST)::value, load = decltype(LOAD)::value;
        f32x16 dn;
        auto fm = [&](int j0, int nn) {
          if (!first) {
#pragma unroll
            for (int j = j0; j < j0 + nn; ++j) { float a_ = aa[j & 15]; asm volatile("v_fmac_f32 %0, %1, %2" : "+v"(a_) : "v"(nm[j >> 4][j & 15]), "v"(inv[j >> 4])); aa[j & 15] = a_; }
          }
        };
        auto ll = [&](int t) { if (load) l1(fn, zn, hgn, t); };
        mf0(dn, z[0], f.b[0]); ll(0); SCHED(); fm(0, 8); SCHED();
        mfa(dn, z[1], f.b[1]); ll(1); SCHED(); fm(8, 8); SCHED();
        mf0(nm[0], f.a[0], f.b[0]); ll(2); SCHED(); fm(16, 8); SCHED();
        mfa(dn, z[2], f.b[2]); ll(3); SCHED(); fm(24, 8); SCHED();
        mf0(nm[1], f.a[1], f.b[1]); SCHED(); fm(32, 8); SCHED();
        mfa(dn, z[3], f.b[3]); SCHED(); fm(40, 8); SCHED();
        mf0(nm[2], f.a[2], f.b[2]); SCHED(); fm(48, 16); SCHED();
        mf0(nm[3], f.a[3], f.b[3]);
        if (first) asm volatile("s_nop 15" : "+v"(dn)); else asm volatile("s_nop 3" : "+v"(dn));
#pragma unroll
        for (int t = 0; t < 4; ++t) inv[t] = __builtin_amdgcn_rcpf(dn[t] + 1e-6f);
        asm volatile("" : "+v"(inv[0]), "+v"(inv[1]), "+v"(inv[2]), "+v"(inv[3]));
        if (load) ++tc;
      };
      GAS unsigned* const uflag = (GAS unsigned*)(ws + WS_UFLAG) + (jj_ * 32 + ft) * 32;
      if (PUB_U == 0 && !isR) publish(p, cw, C_U, uflag, nullptr, lane);
      BAR(); SCHED();
#pragma unroll
      for (int t = 0; t < 4; ++t) l1(f0, z0, 0, t);
      ++tc;
      SCHED(); LGKM0();
      if (PUB_U == 1 && !isR) publish(p, cw, C_U, uflag, nullptr, lane);
      BAR(); SCHED(); step(f0, z0, std::true_type{}, std::true_type{}, f1, z1, 1); SCHED(); LGKM0();
      if (PUB_U == 2 && !isR) publish(p, cw, C_U, uflag, nullptr, lane);
#pragma unroll 1
      for (int i = 2; i < 8; i += 2) {
        BAR(); SCHED(); step(f1, z1, std::false_type{}, std::true_type{}, f0, z0, i); SCHED(); LGKM0();
        BAR(); SCHED(); step(f0, z0, std::false_type{}, std::true_type{}, f1, z1, i + 1); SCHED(); LGKM0();
      }
      step(f1, z1, std::false_type{}, std::false_type{}, f0, z0, 0);
      asm volatile("s_nop 15\n\ts_nop 7" : "+v"(nm[0]), "+v"(nm[1]), "+v"(nm[2]), "+v"(nm[3]));
#pragma unroll
      for (int t = 0; t < 4; ++t)
#pragma unroll
        for (int k = 0; k < 16; ++k) aa[k] = __builtin_fmaf(nm[t][k], inv[t], aa[k]);
      asm volatile("" : "+v"(aa));
    }
    stamp(p, w == 0 ? 6 : 63);
    const int s = st * 64 + 32 * qb + (lane & 31);
    const float b2 = bflo((unsigned)p.b2[s]);
    f32x16 ay = {};
    float g = 0.f;
    kloop<28, false, K_H, K_T>(ay, lds, tc, offH(qa), offT(qb), [&](int i) {
      if (i == 4) g = __builtin_bit_cast(float, __builtin_amdgcn_raw_buffer_load_b32(rsW, (unsigned)(WS_G + s * 4), 0, 16)); });
    stamp(p, w == 0 ? 8 : 63);
    const float g1 = 1.f - g;
    GAS char* ob = (GAS char*)p.out + (size_t)s * 1024 + (jj_ * 64 + 32 * qa) * 2;
#pragma unroll
    for (int kp = 0; kp < 2; ++kp) {
      float v[8];
#pragma unroll
      for (int i = 0; i < 8; ++i) v[i] = g * aa[8 * kp + i] + g1 * (ay[8 * kp + i] + b2);
      unsigned a0 = pk2(v[0], v[1]), a1 = pk2(v[2], v[3]), b0 = pk2(v[4], v[5]), b1 = pk2(v[6], v[7]);
      auto r0 = __builtin_amdgcn_permlane32_swap(a0, b0, false, false);
      auto r1 = __builtin_amdgcn_permlane32_swap(a1, b1, false, false);
      u32x4 o = {r0[0], r1[0], r0[1], r1[1]};
      *(GAS u32x4*)(ob + kp * 32 + h * 16) = o;
    }
    stamp(p, w == 0 ? 10 : 63);
  }
}

extern "C" void kernel_launch(void* const* d_in, const int* in_sizes, int n_in, void* d_out, int out_size, void* d_ws, size_t ws_size,
                              hipStream_t stream) {
  Params p;
  p.x = (const unsigned short*)d_in[0]; p.wqkv = (const unsigned short*)d_in[1]; p.wo = (const unsigned short*)d_in[2];
  p.w1 = (const unsigned short*)d_in[4]; p.b1 = (const unsigned short*)d_in[5]; p.w2 = (const unsigned short*)d_in[6];
  p.b2 = (const unsigned short*)d_in[7]; p.gw = (const unsigned short*)d_in[8];
  p.out = (unsigned short*)d_out; p.ws = (char*)d_ws;
  (void)hipFuncSetAttribute((const void*)fused_k, hipFuncAttributeMaxDynamicSharedMemorySize, L_BYTES);
  fused_k<<<256, 448 + 64 * PROF, L_BYTES, stream>>>(p);
}
```

```cpp
#include <hip/hip_runtime.h>
#include <hip/hip_bf16.h>
#include <stdint.h>
#include <type_traits>

typedef __attribute__((ext_vector_type(8))) __bf16 bf16x8;
typedef __attribute__((ext_vector_type(2))) __bf16 bf16x2;
typedef __attribute__((ext_vector_type(4))) short s16x4;
typedef __attribute__((ext_vector_type(16))) float f32x16;
typedef __attribute__((ext_vector_type(4))) float f32x4;
typedef __attribute__((ext_vector_type(2))) float f32x2;
typedef __attribute__((ext_vector_type(4))) unsigned u32x4;
typedef __attribute__((ext_vector_type(2))) unsigned u32x2;
#define LAS __attribute__((address_space(3)))
#define GAS __attribute__((address_space(1)))
#define DEVI __device__ __forceinline__

#define PROF 0
#define STIME "s_memrealtime"
#define UOFF 0
#define W2OFF 0
#define W2STEP 1
#define W1STEP 1
#define HSTEP 1
#define FA_V 8
#define HFROM 5
#define PF1 8
#define PF0 0
#define PF0SLEEP 16
#define PF1N 1
#define PF2 7
#define PPER 4
#define PSLEEP 8
#define PUB_L 1
#define PUB_P 2
#define PUB_U 1

constexpr size_t WS_EPOCH = 0;
constexpr size_t WS_TMO   = 128;
constexpr size_t WS_HFLAG = 4096;
constexpr size_t WS_RFLAG = 12288;
constexpr size_t WS_PFLAG = 16384;
constexpr size_t WS_UFLAG = 49152;
constexpr size_t WS_QFLAG = 81920;
constexpr size_t WS_FEND  = 114688;
constexpr size_t WS_G     = 114688;
constexpr size_t WS_Z     = 122880;
constexpr size_t WS_H     = 131072;
constexpr size_t WS_PART  = WS_H + 2097152;
constexpr size_t WS_U     = WS_PART + 32 * 32 * 1152;
constexpr size_t WS_PQ    = WS_U + 8 * 28 * 8192;
constexpr size_t WS_KW    = WS_PQ + 32 * 8 * 8192;
constexpr size_t WS_DBG   = WS_KW + 32 * 16384;
constexpr size_t WS_DBG2  = WS_DBG + 256 * 64 * 8;
constexpr size_t WS_END   = WS_DBG2 + 256 * 128 * 8;
constexpr int RING = 8;
constexpr int L_RW = 0, L_RA = RING * 8192, L_CW = 2 * RING * 8192;
constexpr int L_MIR = L_CW + 256;
constexpr int L_ZT = L_MIR + 1024 + 3 * 128;
constexpr int L_ZERO = L_ZT + 1024;
constexpr int L_PFS = L_ZERO + 64;
constexpr int L_RS = L_PFS + 256;
constexpr int L_BYTES = L_RS + 4 * 5120;
enum { W_TAG = 0, W_GATE = 4, W_CNT = 12 };
enum { G_H = 0, G_PQ = 1, G_R = 2, G_U = 3 };
enum { C_L = 0, C_PART = 1, C_U = 2, C_R = 3, C_RX = 4 };
constexpr unsigned SPIN_LDS = 1u << 19, SPIN_GLB = 1u << 15;

typedef volatile LAS unsigned* cw_t;

struct Params {
  const unsigned short *x, *wqkv, *wo, *w1, *b1, *w2, *b2, *gw;
  unsigned short* out;
  char* ws;
};

DEVI unsigned rfl(unsigned v) { return __builtin_amdgcn_readfirstlane(v); }
DEVI unsigned pk2(float a, float b) { f32x2 v = {a, b}; return __builtin_bit_cast(unsigned, __builtin_convertvector(v, bf16x2)); }
DEVI float bflo(unsigned u) { return __builtin_bit_cast(float, u << 16); }
DEVI float bfhi(unsigned u) { return __builtin_bit_cast(float, u & 0xffff0000u); }
DEVI f32x16 mfma32(bf16x8 a, bf16x8 b, f32x16 c) { return __builtin_amdgcn_mfma_f32_32x32x16_bf16(a, b, c, 0, 0, 0); }
DEVI bf16x8 lds_r(LAS char* p) { return *(LAS bf16x8*)p; }
DEVI bf16x8 lds_t(LAS char* p) {
  s16x4 a = __builtin_amdgcn_ds_read_tr16_b64_v4i16((LAS s16x4*)p);
  s16x4 b = __builtin_amdgcn_ds_read_tr16_b64_v4i16((LAS s16x4*)(p + 512));
  return __builtin_bit_cast(bf16x8, __builtin_shufflevector(a, b, 0, 1, 2, 3, 4, 5, 6, 7));
}
template <int CTRL> DEVI float dpp_add(float v) {
  return v + __builtin_bit_cast(float, __builtin_amdgcn_update_dpp(0, __builtin_bit_cast(int, v), CTRL, 0xf, 0xf, true));
}
DEVI float wave_sum(float v) {
  v = dpp_add<0xB1>(v); v = dpp_add<0x4E>(v); v = dpp_add<0x141>(v); v = dpp_add<0x140>(v);
  int iv = __builtin_bit_cast(int, v);
  float a = __builtin_bit_cast(float, __builtin_amdgcn_readlane(iv, 0)), b = __builtin_bit_cast(float, __builtin_amdgcn_readlane(iv, 16));
  float c = __builtin_bit_cast(float, __builtin_amdgcn_readlane(iv, 32)), d = __builtin_bit_cast(float, __builtin_amdgcn_readlane(iv, 48));
  return (a + b) + (c + d);
}
DEVI float xhalf(float v) {
  unsigned u = __builtin_bit_cast(unsigned, v);
  auto r = __builtin_amdgcn_permlane32_swap(u, u, false, false);
  return __builtin_bit_cast(float, (threadIdx.x & 32) ? r[0] : r[1]);
}
DEVI float phi(float t) {
  const float e = __builtin_amdgcn_exp2f(__builtin_fminf(t * 1.4426950408889634f, 0.f));
  return (__builtin_fmaxf(t, 0.f) + 1e-6f) + e;
}
DEVI float gelu_t(float u) {
  const float k1 = -1.5957691216057308f * 1.4426950408889634f, k3 = k1 * 0.044715f;
  const float e = __builtin_amdgcn_exp2f(u * __builtin_fmaf(u * u, k3, k1));
  return u * __builtin_amdgcn_rcpf(1.f + e);
}
#define VM0() asm volatile("s_waitcnt vmcnt(0)" ::: "memory")
#define VMN(n) asm volatile("s_waitcnt vmcnt(" #n ")" ::: "memory")
#define LGKM0() asm volatile("s_waitcnt lgkmcnt(0)" ::: "memory")
#define BAR() asm volatile("s_barrier" ::: "memory")
#define SCHED() __builtin_amdgcn_sched_barrier(0)
#define RLX_AGENT __ATOMIC_RELAXED, __HIP_MEMORY_SCOPE_AGENT

DEVI void stamp(const Params& p, int i) {
}

template <int SLP>
DEVI void poll_blocking(__amdgpu_buffer_rsrc_t rs, unsigned off, int n, bool wide, unsigned tag, int lane, char* ws) {
  const unsigned o = off + (unsigned)(lane < n ? lane : n - 1) * 128u;
  for (unsigned sp = 0;; ++sp) {
    bool ok;
    if (wide) { const u32x4 v = __builtin_amdgcn_raw_buffer_load_b128(rs, o, 0, 16); ok = v.x == tag && v.y == tag && v.z == tag && v.w == tag; }
    else ok = __builtin_amdgcn_raw_buffer_load_b32(rs, o, 0, 16) == tag;
    if (__all(ok)) break;
    if (sp > SPIN_GLB) { __hip_atomic_store((GAS unsigned*)(ws + WS_TMO), 7u, RLX_AGENT); break; }
    __builtin_amdgcn_s_sleep(SLP);
  }
  asm volatile("" ::: "memory");
}

template <bool SC1>
DEVI void dma8(unsigned m0v, const char* b0, const char* b1, unsigned v0, unsigned v1, unsigned v2, unsigned v3) {
  constexpr bool USE_SC1 = SC1;
  if (USE_SC1)
    asm volatile("s_mov_b32 m0, %0\n\ts_nop 0\n\t"
                 "global_load_lds_dwordx4 %1, %5 sc1\n\t"
                 "global_load_lds_dwordx4 %2, %5 offset:1024 sc1\n\t"
                 "global_load_lds_dwordx4 %3, %5 offset:2048 sc1\n\t"
                 "global_load_lds_dwordx4 %4, %5 offset:3072 sc1\n\t"
                 "s_add_u32 m0, m0, 0x1000\n\ts_nop 0\n\t"
                 "global_load_lds_dwordx4 %1, %6 sc1\n\t"
                 "global_load_lds_dwordx4 %2, %6 offset:1024 sc1\n\t"
                 "global_load_lds_dwordx4 %3, %6 offset:2048 sc1\n\t"
                 "global_load_lds_dwordx4 %4, %6 offset:3072 sc1"
                 :: "s"(m0v), "v"(v0), "v"(v1), "v"(v2), "v"(v3), "s"(b0), "s"(b1) : "memory", "scc");
  else
    asm volatile("s_mov_b32 m0, %0\n\ts_nop 0\n\t"
                 "global_load_lds_dwordx4 %1, %5\n\t"
                 "global_load_lds_dwordx4 %2, %5 offset:1024\n\t"
                 "global_load_lds_dwordx4 %3, %5 offset:2048\n\t"
                 "global_load_lds_dwordx4 %4, %5 offset:3072\n\t"
                 "s_add_u32 m0, m0, 0x1000\n\ts_nop 0\n\t"
                 "global_load_lds_dwordx4 %1, %6\n\t"
                 "global_load_lds_dwordx4 %2, %6 offset:1024\n\t"
                 "global_load_lds_dwordx4 %3, %6 offset:2048\n\t"
                 "global_load_lds_dwordx4 %4, %6 offset:3072"
                 :: "s"(m0v), "v"(v0), "v"(v1), "v"(v2), "v"(v3), "s"(b0), "s"(b1) : "memory", "scc");
}
DEVI void wait_vm_tiles(int n) {
  if (n >= 4) { if (n >= 6) { if (n >= 7) VMN(56); else VMN(48); } else { if (n == 5) VMN(40); else VMN(32); } }
  else { if (n >= 2) { if (n == 3) VMN(24); else VMN(16); } else { if (n == 1) VMN(8); else VM0(); } }
}
DEVI int used(int i) { return i < 8 ? 2 * i + 2 : i + 9; }
struct Loader {
  cw_t cw; char* ws; __amdgpu_buffer_rsrc_t rsF; unsigned ldsb; int I, i, lane;
  template <bool SC1> DEVI void issue(const char* base, long half, unsigned v0, unsigned v1, unsigned v2, unsigned v3) {
    dma8<SC1>(ldsb + (unsigned)(I & (RING - 1)) * 8192u, base, base + half, v0, v1, v2, v3); ++I;
  }
  DEVI void ztable() {
    asm volatile("s_mov_b32 m0, %0\n\ts_nop 0\n\tglobal_load_lds_dwordx4 %1, %2 sc1" :: "s"((unsigned)L_ZT), "v"((unsigned)lane * 16u), "s"(ws + WS_Z) : "memory");
  }
  template <bool SC1, bool ZT>
  DEVI void stream(int end, const char* base, long step, long half, unsigned v0, unsigned v1, unsigned v2, unsigned v3, int gate, unsigned foff, int fn) {
    if (gate >= 0) {
      for (;;) {
        const unsigned gw = *(LAS unsigned*)((LAS char*)cw + (W_GATE + gate) * 4);
        const int d = I - used(i);
        if (d < 0) {
          if (rfl(gw) == 0u) {
            poll_blocking<PSLEEP>(rsF, foff, fn, gate == G_H, cw[W_TAG], lane, ws); if (lane == 0) cw[W_GATE + gate] = 1u;
          }
          break;
        }
        wait_vm_tiles(d); BAR(); ++i;
        if (rfl(gw) != 0u) break;
      }
      if (ZT) ztable();
      const int lim = used(i - 2) + RING;
      while (I < lim && I < end) { issue<SC1>(base, half, v0, v1, v2, v3); base += step; }
    }
    while (I < end) {
      if (i >= 9 && I == i + 7 + RING) {
        const int n0 = end - I;
#pragma unroll 1
        for (int n = n0; n > 0; --n) { VMN(48); BAR(); issue<SC1>(base, half, v0, v1, v2, v3); base += step; }
        i += n0;
        break;
      }
      wait_vm_tiles(I - used(i)); BAR();
      const int lim = used(i - 1) + RING; ++i;
      while (I < lim && I < end) { issue<SC1>(base, half, v0, v1, v2, v3); base += step; }
    }
  }
  DEVI void tail(int ntot) { while (i < ntot) { wait_vm_tiles(I - used(i)); BAR(); ++i; } }
};

struct Frag { bf16x8 a[4], b[4]; };
DEVI void mf0(f32x16& d, bf16x8 a, bf16x8 b) { asm volatile("v_mfma_f32_32x32x16_bf16 %0, %1, %2, 0" : "=&v"(d) : "v"(a), "v"(b)); }
DEVI void mfa(f32x16& d, bf16x8 a, bf16x8 b) { asm volatile("v_mfma_f32_32x32x16_bf16 %0, %1, %2, %0" : "+v"(d) : "v"(a), "v"(b)); }
enum { K_T = 0, K_H = 2 };
template <int KIND> DEVI bf16x8 ld_frag1(LAS char* slot, unsigned off, int t) {
  if (KIND == K_T) return lds_t(slot + off + t * 2048);
  else return lds_r(slot + (off ^ ((t & 1) << 5)) + (t >> 1) * 4096);
}
template <int KA, int KB>
DEVI void load_frag(Frag& f, LAS char* sa, LAS char* sb, unsigned offA, unsigned offB) {
#pragma unroll
  for (int t = 0; t < 4; ++t) { f.a[t] = ld_frag1<KA>(sa, offA, t); f.b[t] = ld_frag1<KB>(sb, offB, t); }
}
DEVI void mma4(f32x16& acc, const Frag& f) {
#pragma unroll
  for (int t = 0; t < 4; ++t) acc = mfma32(f.a[t], f.b[t], acc);
}
DEVI void interleave() {
  __builtin_amdgcn_sched_group_barrier(0x100, 6, 0); __builtin_amdgcn_sched_group_barrier(0x8, 1, 0);
  __builtin_amdgcn_sched_group_barrier(0x100, 6, 0); __builtin_amdgcn_sched_group_barrier(0x8, 1, 0);
  __builtin_amdgcn_sched_group_barrier(0x100, 4, 0); __builtin_amdgcn_sched_group_barrier(0x8, 2, 0);
}
template <int NT, bool A_FROM_W, int KA, int KB, class Hook>
DEVI void kloop(f32x16& acc, LAS char* lds, int& tc, unsigned offA, unsigned offB, Hook hook) {
  Frag f0, f1;
  auto ld = [&](Frag& f) {
    const unsigned sw = (unsigned)(tc & (RING - 1)) * 8192u + L_RW, sa = sw + (L_RA - L_RW);
    const unsigned s1 = A_FROM_W ? sw : sa, s2 = A_FROM_W ? sa : sw;
    load_frag<KA, KB>(f, lds + s1, lds + s2, offA, offB);
    ++tc;
  };
  BAR(); SCHED();
  ld(f0);
  SCHED(); LGKM0();
  hook(0);
#pragma unroll 1
  for (int i = 1; i < NT - 1; i += 2) {
    BAR(); SCHED();
    ld(f1); mma4(acc, f0); interleave();
    SCHED(); LGKM0();
    hook(i);
    BAR(); SCHED();
    ld(f0); mma4(acc, f1); interleave();
    SCHED(); LGKM0();
    hook(i + 1);
  }
  BAR(); SCHED();
  ld(f1); mma4(acc, f0); interleave();
  asm volatile("" : "+v"(acc));
  SCHED(); LGKM0();
  hook(NT - 1);
  mma4(acc, f1);
  asm volatile("" : "+v"(acc));
}
DEVI void store_quadrant(const float (&v)[16], __amdgpu_buffer_rsrc_t rs, unsigned tbase, int qa, int qb, int lane) {
  unsigned x[2][4];
#pragma unroll
  for (int kp = 0; kp < 2; ++kp) {
    const int g = 2 * kp;
    unsigned a0 = pk2(v[4 * g + 0], v[4 * g + 1]), a1 = pk2(v[4 * g + 2], v[4 * g + 3]);
    unsigned b0 = pk2(v[4 * g + 4], v[4 * g + 5]), b1 = pk2(v[4 * g + 6], v[4 * g + 7]);
    auto r0 = __builtin_amdgcn_permlane32_swap(a0, b0, false, false);
    auto r1 = __builtin_amdgcn_permlane32_swap(a1, b1, false, false);
    x[kp][0] = r0[0]; x[kp][1] = r1[0]; x[kp][2] = r0[1]; x[kp][3] = r1[1];
  }
  u32x4 y0, y1;
#pragma unroll
  for (int i = 0; i < 4; ++i) { auto r = __builtin_amdgcn_permlane16_swap(x[0][i], x[1][i], false, false); y0[i] = r[0]; y1[i] = r[1]; }
  const unsigned row = lane & 15, c = 2u * ((lane >> 4) & 1) + (lane >> 5);
  const unsigned o = tbase + qa * 4096u + (32u * qb + row) * 64u + ((c ^ (row >> 2)) << 4);
  __builtin_amdgcn_raw_buffer_store_b128(y0, rs, o, 0, 16);
  __builtin_amdgcn_raw_buffer_store_b128(y1, rs, o + 1024u, 0, 16);
}
DEVI void publish(const Params& p, cw_t cw, int cnt, GAS unsigned* flag, GAS unsigned* flag2, int lane) {
  VM0();
  if (lane == 0) {
    unsigned old = __hip_atomic_fetch_add((LAS unsigned*)(cw + W_CNT + cnt), 1u, __ATOMIC_RELAXED, __HIP_MEMORY_SCOPE_WORKGROUP);
    if (old == 3u) { const unsigned tg = cw[W_TAG]; __hip_atomic_store(flag, tg, RLX_AGENT); if (flag2) __hip_atomic_store(flag2, tg, RLX_AGENT);
    }
  }
}

__global__ void __launch_bounds__(448 + 64 * PROF, 2) fused_k(Params p) {
  extern __shared__ __attribute__((aligned(16))) char shm[];
  LAS char* lds = (LAS char*)shm;
  cw_t cw = (cw_t)(lds + L_CW);
  const int tid = threadIdx.x, lane = tid & 63;
  const int wid = __builtin_amdgcn_readfirstlane(tid >> 6);
  const int b = blockIdx.x, xcd = b & 7, idx = b >> 3;
  const int st = (xcd >> 1) * 8 + (idx >> 2), jj_ = (xcd & 1) * 4 + (idx & 3);
  const bool isR = idx >= 28;
  const int ft = (xcd >> 1) * 7 + (idx >> 2);
  const int rh = xcd * 4 + (idx & 3);
  const int NG1 = isR ? 0 : 32;
  const int NTOT = 8 + NG1 + 8 + 28;
  char* ws = p.ws;

  if (wid == 4 || wid == 5) {
    const int isA = wid - 4;
    const int rr = lane >> 3, cc = lane & 7;
    Loader L; L.cw = cw; L.ws = ws; L.lane = lane; L.ldsb = (unsigned)(isA ? L_RA : L_RW); L.I = 0;
    L.rsF = __builtin_amdgcn_make_buffer_rsrc(ws, 0, (int)WS_FEND, 0x00020000);
    __builtin_amdgcn_s_setprio(3);
    const unsigned cT = (unsigned)(cc ^ (((rr >> 1) & 1) << 2));
    const unsigned cR0 = (unsigned)(cc ^ (rr >> 1)), cR1 = cR0 ^ 4u;
    auto vT = [&](int j, unsigned sb, unsigned c) { return (unsigned)(8 * j + rr) * sb + c * 16u - j * 1024u; };
    {
      const char* b0; const char* b1; long st0, h0; unsigned p0, p1, p2, p3, x1;
      if (!isA) {
        b0 = (const char*)p.wqkv + (512 + jj_ * 64) * 2; b1 = b0 + 1024; st0 = 64 * 3072; h0 = 32 * 3072;
        p0 = vT(0, 3072, cT); p1 = vT(1, 3072, cT); p2 = vT(2, 3072, cT); p3 = vT(3, 3072, cT); x1 = 2;
      } else {
        b0 = (const char*)p.x + (size_t)(st * 64) * 1024; st0 = 128; h0 = 32 * 1024;
        b1 = (const char*)p.wqkv + jj_ * 128;
        p0 = vT(0, 1024, cR0); p1 = vT(1, 1024, cR1); p2 = vT(2, 1024, cR0); p3 = vT(3, 1024, cR1); x1 = 0;
      }
      const unsigned q0 = vT(0, 3072, cT ^ x1), q1 = vT(1, 3072, cT ^ x1), q2 = vT(2, 3072, cT ^ x1), q3 = vT(3, 3072, cT ^ x1);
      asm volatile("" :: "v"(p0), "v"(p1), "v"(p2), "v"(p3), "v"(q0), "v"(q1), "v"(q2), "v"(q3), "s"(b0), "s"(b1));
      BAR();
      for (int k = 0; k < 4; ++k) { L.issue<false>(b0, h0, p0, p1, p2, p3); L.issue<false>(b1, 32 * 3072, q0, q1, q2, q3); b0 += st0; b1 += 64 * 3072; }
      VMN(48); BAR();
#pragma unroll 1
      for (int k = 1; k <= 4; ++k) {
        VMN(32); BAR();
        L.issue<false>(b0, h0, p0, p1, p2, p3); L.issue<false>(b1, 32 * 3072, q0, q1, q2, q3); b0 += st0; b1 += 64 * 3072;
      }
      L.i = 5;
    }
    stamp(p, isA ? 48 : 32);
    const unsigned l0 = (unsigned)lane * 16u;
    if (!isA) {
      if (!isR) L.stream<false, false>(48, (const char*)p.w1 + ft * 128, W1STEP * 64L * 3584, 32L * 3584, vT(0, 3584, cT), vT(1, 3584, cT), vT(2, 3584, cT), vT(3, 3584, cT), -1, 0, 0);
      stamp(p, 33);
      L.stream<true, true>(16 + NG1 + 8, ws + WS_KW + (size_t)(jj_ * 8) * 8192, 8192, 4096, l0, l0, l0, l0, G_R, (unsigned)WS_RFLAG, 32);
      stamp(p, 34);
      L.stream<false, false>(16 + NG1 + 8 + 28, (const char*)p.w2 + W2OFF + st * 128, W2STEP * 64L * 4096, 32L * 4096, vT(0, 4096, cT), vT(1, 4096, cT), vT(2, 4096, cT), vT(3, 4096, cT), -1, 0, 0);
      stamp(p, 35);
    } else {
      if (!isR) L.stream<true, false>(48, ws + WS_H + (size_t)jj_ * 8192, HSTEP * 8L * 8192, 4096, l0, l0, l0, l0, G_H, (unsigned)WS_HFLAG, 64);
      stamp(p, 49);
      L.stream<true, false>(16 + NG1 + 8, ws + WS_PQ + (size_t)(st * 8) * 8192, 8192, 4096, l0, l0, l0, l0, G_PQ, (unsigned)WS_QFLAG + st * 1024, 8);
      stamp(p, 50);
      L.stream<true, false>(16 + NG1 + 8 + 28, ws + WS_U + UOFF + (size_t)(jj_ * 28) * 8192, 8192, 4096, l0, l0, l0, l0, G_U, (unsigned)WS_UFLAG + jj_ * 4096, 28);
      stamp(p, 51);
    }
    L.tail(NTOT);
    VM0();
    stamp(p, isA ? 52 : 36);
    return;
  }

  if (wid == 6) {
    {
      u32x4 z = {0u, 0u, 0u, 0u};
      *(LAS u32x4*)(lds + L_CW + lane * 16) = z;
      if (lane < 40) *(LAS u32x4*)(lds + L_CW + 1024 + lane * 16) = z;
      if (lane == 63) *(LAS u32x4*)(lds + L_ZERO) = z;
    }
    unsigned tag;
    asm volatile("global_load_dword %0, %1, %2 sc1" : "=v"(tag) : "v"(0u), "s"(ws + WS_EPOCH) : "memory");
    const char* pf1 = (const char*)p.w1 + (size_t)(idx * 64) * 3584 + (xcd >> 1) * 7 * 128;
    const char* pf2 = (const char*)p.w2 + (size_t)(idx * 56) * 4096 + (xcd >> 1) * 1024;
    const unsigned pv1 = (unsigned)lane * 3584u, pv2 = (unsigned)(lane >> 3) * 4096u + (unsigned)(lane & 7) * 128u;
    BAR();
    asm volatile("s_waitcnt vmcnt(%1)" : "+v"(tag) : "i"(PF0) : "memory");
    tag = rfl(tag) + 1u; if (tag == 0u) tag = 1u;
    if (lane == 0) cw[W_TAG] = tag;
    LGKM0();
    int left = NTOT;
    auto prefetch = [&](int i_) {
      const int i = (int)rfl((unsigned)i_);
      if (i >= PF1 && (i - PF1) * PF1N < 7 - PF0) {
        for (int k = PF0 + (i - PF1) * PF1N; k < PF0 + (i - PF1 + 1) * PF1N && k < 7; ++k)
          asm volatile("s_mov_b32 m0, %0\n\ts_nop 0\n\tglobal_load_lds_dword %1, %2" :: "s"((unsigned)L_PFS), "v"(pv1), "s"(pf1 + k * 128) : "memory");
      }
      const int i2 = i - (NG1 + PF2);
      if (i2 >= 0 && i2 < 7) asm volatile("s_mov_b32 m0, %0\n\ts_nop 0\n\tglobal_load_lds_dword %1, %2" :: "s"((unsigned)L_PFS), "v"(pv2), "s"(pf2 + i2 * 8 * 4096) : "memory");
    };
#define PBAR() do { BAR(); prefetch(NTOT - left); } while (0)
    auto gate_loop = [&](int g, const char* fb, unsigned moff, int n, auto WIDE, int from, int pmask) {
#pragma unroll 1
      while (left > NTOT - from) { PBAR(); --left; }
      constexpr bool wide = decltype(WIDE)::value;
      const int lc = lane < n ? lane : n - 1;
      const unsigned voff = (unsigned)lc * 128u;
      const unsigned roff = moff + (unsigned)lc * (wide ? 16u : 4u);
      u32x4 v = {0u, 0u, 0u, 0u}; unsigned gw = 0u;
      while (left > 0) {
        PBAR(); --left;
        if (__all((v.x == tag && v.y == tag && v.z == tag && v.w == tag) || gw != 0u)) {
          if (lane == 0) cw[W_GATE + g] = 1u;
          stamp(p, 24 + g);
          break;
        }
        if ((left & pmask) == 0 && lane < n) {
          if (wide) asm volatile("s_mov_b32 m0, %0\n\ts_nop 0\n\tglobal_load_lds_dwordx4 %1, %2 sc1" :: "s"(moff), "v"(voff), "s"(fb) : "memory");
          else asm volatile("s_mov_b32 m0, %0\n\ts_nop 0\n\tglobal_load_lds_dword %1, %2 sc1" :: "s"(moff), "v"(voff), "s"(fb) : "memory");
        }
        if (wide) v = *(LAS u32x4*)(lds + roff);
        else { const unsigned x = *(LAS unsigned*)(lds + roff); v = (u32x4){x, x, x, x}; }
        gw = *(LAS unsigned*)(lds + L_CW + (W_GATE + g) * 4);
      }
    };
    gate_loop(G_H, ws + WS_HFLAG, L_MIR, 64, std::true_type{}, HFROM, 0);
    if (b == 0 && lane == 0) __hip_atomic_store((GAS unsigned*)(ws + WS_EPOCH), tag, RLX_AGENT);
    gate_loop(G_PQ, ws + WS_QFLAG + st * 1024, L_MIR + 1024, 8, std::false_type{}, 8 + NG1 - 12, PPER - 1);
    gate_loop(G_R, ws + WS_RFLAG, L_MIR + 1024 + 128, 32, std::false_type{}, 8 + NG1 - 10, PPER - 1);
    gate_loop(G_U, ws + WS_UFLAG + jj_ * 4096, L_MIR + 1024 + 256, 28, std::false_type{}, 8 + NG1 + 1, 0);
#pragma unroll 1
    for (; left > 0; --left) PBAR();
    VM0();
    return;
  }

  const int w = wid, qa = w & 1, qb = w >> 1, h = lane >> 5;
  const __amdgpu_buffer_rsrc_t rsW = __builtin_amdgcn_make_buffer_rsrc(ws, 0, (int)WS_END, 0x00020000);
  stamp(p, w == 0 ? 0 : 63);
  {
    const int rl = jj_ * 8 + 2 * w, r0 = st * 64 + rl;
    u32x4 xv[2];
    xv[0] = *(const GAS u32x4*)((const GAS char*)p.x + (size_t)r0 * 1024 + lane * 16);
    xv[1] = *(const GAS u32x4*)((const GAS char*)p.x + (size_t)(r0 + 1) * 1024 + lane * 16);
    const u32x4 gv = *(const GAS u32x4*)((const GAS char*)p.gw + lane * 16);
    BAR();
    u32x4 o[2]; float gt[2];
#pragma unroll
    for (int r = 0; r < 2; ++r) {
      float x[8], s = 0.f, gd = 0.f;
#pragma unroll
      for (int i = 0; i < 4; ++i) { x[2 * i] = bflo(xv[r][i]); x[2 * i + 1] = bfhi(xv[r][i]); gd += x[2 * i] * bflo(gv[i]) + x[2 * i + 1] * bfhi(gv[i]); }
#pragma unroll
      for (int i = 0; i < 8; ++i) s += x[i];
      const float m = wave_sum(s) * (1.f / 512.f);
      float q = 0.f;
#pragma unroll
      for (int i = 0; i < 8; ++i) { x[i] -= m; q += x[i] * x[i]; }
      const float rs = __builtin_amdgcn_rsqf(wave_sum(q) * (1.f / 512.f) + 1e-5f);
      gt[r] = __builtin_amdgcn_rcpf(1.f + __expf(-wave_sum(gd)));
      o[r] = (u32x4){pk2(x[0] * rs, x[1] * rs), pk2(x[2] * rs, x[3] * rs), pk2(x[4] * rs, x[5] * rs), pk2(x[6] * rs, x[7] * rs)};
    }
    asm volatile("" : "+v"(o[0]), "+v"(o[1]), "+v"(gt[0]), "+v"(gt[1]));
#pragma unroll
    for (int r = 0; r < 2; ++r) {
      const int row = rl + r;
      const unsigned cs = (unsigned)(lane & 7) ^ (unsigned)(((row >> 1) & 1) << 2);
      __builtin_amdgcn_raw_buffer_store_b128(o[r], rsW, (unsigned)WS_H + (unsigned)((st * 8 + (lane >> 3)) * 8192 + row * 128) + cs * 16u, 0, 16);
    }
    if (lane == 0) { f32x2 gg = {gt[0], gt[1]}; __builtin_amdgcn_raw_buffer_store_b64(__builtin_bit_cast(u32x2, gg), rsW, (unsigned)(WS_G + r0 * 4), 0, 16); }
  }
  stamp(p, w == 0 ? 1 : 63);

  const int blk = (lane >> 4) & 1, qq = (lane & 15) >> 2, pp = lane & 3;
  const unsigned row0 = 8 * h + qq, swT = ((qq >> 1) & 1) << 2;
  auto offT = [&](int q) { return row0 * 128u + (((unsigned)(4 * q + 2 * blk + (pp >> 1)) ^ swT) << 4) + 8u * (pp & 1); };
  auto offR = [&](int q) { return (unsigned)(32 * q + (lane & 31)) * 128u + (((unsigned)h ^ ((lane >> 1) & 7u)) << 4); };
  auto offH = [&](int q) { return (unsigned)(32 * q + (lane & 31)) * 64u + (((unsigned)h ^ ((lane >> 2) & 3u)) << 4); };
  int tc = 0;

  {
    f32x16 a0 = {}, a1 = {}, aq = {};
    const unsigned oB = blk * 8192u + row0 * 128u + (((unsigned)(2 * w + (pp >> 1)) ^ swT ^ (unsigned)(blk << 1)) << 4) + 8u * (pp & 1);
    const unsigned oA0 = offR(qb), oA1 = offR(1 - qb), oQ = 8192u + offT(qa);
    struct F0 { bf16x8 a0[4], a1[4], b[4], q[4]; };
    F0 f0, f1;
    auto ld = [&](F0& f) {
      const unsigned sl = (unsigned)(tc & (RING - 1)) * 8192u;
      LAS char* sw = lds + sl + L_RW + oB;
      LAS char* sa = lds + sl + L_RA;
#pragma unroll
      for (int t = 0; t < 4; ++t) { f.b[t] = lds_t(sw + t * 2048); f.a0[t] = lds_r(sa + (oA0 ^ (t << 5))); f.a1[t] = lds_r(sa + (oA1 ^ (t << 5))); f.q[t] = lds_t(sa + oQ + t * 2048); }
      tc += 2;
    };
    auto mm = [&](const F0& f) {
#pragma unroll
      for (int t = 0; t < 4; ++t) { a0 = mfma32(f.a0[t], f.b[t], a0); a1 = mfma32(f.a1[t], f.b[t], a1); aq = mfma32(f.q[t], f.a0[t], aq); }
#pragma unroll
      for (int t = 0; t < 8; ++t) { __builtin_amdgcn_sched_group_barrier(0x100, 3, 0); __builtin_amdgcn_sched_group_barrier(0x8, 1, 0); }
      __builtin_amdgcn_sched_group_barrier(0x8, 4, 0);
    };
    BAR(); SCHED(); ld(f0); SCHED(); LGKM0();
#pragma unroll 1
    for (int i = 1; i < 7; i += 2) {
      BAR(); SCHED(); ld(f1); mm(f0); SCHED(); LGKM0();
      if (i == 1) publish(p, cw, C_L, (GAS unsigned*)(ws + WS_HFLAG) + (b >> 2) * 32 + (b & 3), nullptr, lane);
      BAR(); SCHED(); ld(f0); mm(f1); SCHED(); LGKM0();
    }
    BAR(); SCHED(); ld(f1); mm(f0); asm volatile("" : "+v"(a0), "+v"(a1), "+v"(aq)); SCHED(); LGKM0();
    mm(f1);
    asm volatile("" : "+v"(a0), "+v"(a1), "+v"(aq));
    stamp(p, w == 0 ? 2 : 63);
    {
      float v[16];
#pragma unroll
      for (int i = 0; i < 16; ++i) v[i] = phi(aq[i]);
      store_quadrant(v, rsW, (unsigned)(WS_PQ + (size_t)(st * 8 + jj_) * 8192), qa, qb, lane);
    }
    const bool isK = blk == 0;
    f32x16 gr = {};
#pragma unroll
    for (int q = 0; q < 2; ++q) {
#pragma unroll
      for (int ss = 0; ss < 2; ++ss) {
        u32x4 fa, fbv;
#pragma unroll
        for (int i = 0; i < 4; ++i) {
          float v0 = q ? a1[8 * ss + 2 * i] : a0[8 * ss + 2 * i], v1 = q ? a1[8 * ss + 2 * i + 1] : a0[8 * ss + 2 * i + 1];
          const float p0 = phi(v0), p1 = phi(v1);
          v0 = isK ? p0 : v0; v1 = isK ? p1 : v1;
          fa[i] = pk2(v0, v1);
          fbv[i] = isK ? 0x3F803F80u : fa[i];
        }
        gr = mfma32(__builtin_bit_cast(bf16x8, fa), __builtin_bit_cast(bf16x8, fbv), gr);
      }
    }
    const unsigned rec = (unsigned)(WS_PART + ((size_t)(jj_ * 4 + w) * 32 + st) * 1152);
    if (blk == 1 || (lane & 31) == 0) {
#pragma unroll
      for (int g = 0; g < 2; ++g) {
        f32x4 v = {gr[4 * g], gr[4 * g + 1], gr[4 * g + 2], gr[4 * g + 3]};
        const unsigned o = blk == 1 ? (unsigned)((g * 2 + h) * 256 + (lane & 15) * 16) : (unsigned)(1024 + (g * 2 + h) * 16);
        __builtin_amdgcn_raw_buffer_store_b128(__builtin_bit_cast(u32x4, v), rsW, rec + o, 0, 16);
      }
    }
    stamp(p, w == 0 ? 3 : 63);
  }
  GAS unsigned* const pflag = (GAS unsigned*)(ws + WS_PFLAG) + (jj_ * 32 + st) * 32;
  GAS unsigned* const qflag = (GAS unsigned*)(ws + WS_QFLAG) + (st * 8 + jj_) * 32;

  if (!isR) {
    if (PUB_P < 0) publish(p, cw, C_PART, pflag, qflag, lane);
    f32x16 acc;
#pragma unroll
    for (int g = 0; g < 4; ++g) {
      const u32x2 bb = *(const GAS u32x2*)((const GAS char*)p.b1 + (ft * 64 + 32 * qa + 8 * g + 4 * h) * 2);
      acc[4 * g + 0] = bflo(bb.x); acc[4 * g + 1] = bfhi(bb.x); acc[4 * g + 2] = bflo(bb.y); acc[4 * g + 3] = bfhi(bb.y);
    }
    kloop<32, true, K_T, K_T>(acc, lds, tc, offT(qa), offT(qb), [&](int i) { if (i == PUB_P) publish(p, cw, C_PART, pflag, qflag, lane); });
    stamp(p, w == 0 ? 4 : 63);
    float v[16];
#pragma unroll
    for (int k = 0; k < 16; ++k) v[k] = gelu_t(acc[k]);
    store_quadrant(v, rsW, (unsigned)(WS_U + (size_t)(jj_ * 28 + ft) * 8192), qa, qb, lane);
    stamp(p, w == 0 ? 5 : 63);
  } else {
    publish(p, cw, C_PART, pflag, qflag, lane);
    LAS char* sc = lds + L_RS + w * 5120;
#pragma unroll
    for (int i = 0; i < 4; ++i) {
      const int e = i * 4 + (lane >> 4);
      const u32x4 wv = *(const GAS u32x4*)((const GAS char*)p.wo + (size_t)(rh * 16 + e) * 1024 + w * 256 + (lane & 15) * 16);
      *(LAS u32x4*)(sc + e * 256 + (lane & 15) * 16) = wv;
    }
    stamp(p, w == 0 ? 20 : 63);
    poll_blocking<1>(rsW, (unsigned)WS_PFLAG + (rh >> 2) * 4096, 32, false, cw[W_TAG], lane, ws);
    stamp(p, w == 0 ? 21 : 63);
    const unsigned pb = (unsigned)(WS_PART + (size_t)rh * 32 * 1152);
    f32x4 kv, zz;
    {
      u32x4 pv[8];
#pragma unroll
      for (int t = 0; t < 8; ++t) pv[t] = __builtin_amdgcn_raw_buffer_load_b128(rsW, pb + (w * 8 + t) * 1152 + lane * 16, 0, 16);
      const u32x4 z0 = __builtin_amdgcn_raw_buffer_load_b128(rsW, pb + (w * 8 + ((lane >> 2) & 7)) * 1152 + 1024 + (lane & 3) * 16, 0, 16);
      kv = __builtin_bit_cast(f32x4, pv[0]);
#pragma unroll
      for (int t = 1; t < 8; ++t) kv += __builtin_bit_cast(f32x4, pv[t]);
      zz = __builtin_bit_cast(f32x4, z0);
#pragma unroll
      for (int k = 0; k < 4; ++k) { float v = zz[k]; v += __shfl_xor(v, 4); v += __shfl_xor(v, 8); v += __shfl_xor(v, 16); zz[k] = v; }
      LAS f32x4* ex = (LAS f32x4*)(lds + L_RW + 32768);
      ex[w * 68 + lane] = kv;
      if (lane < 4) ex[w * 68 + 64 + lane] = zz;
      LGKM0();
      if (lane == 0) (void)__hip_atomic_fetch_add((LAS unsigned*)(cw + W_CNT + C_RX), 1u, __ATOMIC_RELAXED, __HIP_MEMORY_SCOPE_WORKGROUP);
      for (unsigned sp = 0; cw[W_CNT + C_RX] < 4u && sp < SPIN_LDS; ++sp) __builtin_amdgcn_s_sleep(1);
      asm volatile("" ::: "memory");
      kv = (ex[lane] + ex[68 + lane]) + (ex[136 + lane] + ex[204 + lane]);
      zz = (ex[64 + (lane & 3)] + ex[132 + (lane & 3)]) + (ex[200 + (lane & 3)] + ex[268 + (lane & 3)]);
    }
    asm volatile("" : "+v"(kv), "+v"(zz));
    stamp(p, w == 0 ? 22 : 63);
    {
      const int e = lane & 15, d0 = (lane >> 5) * 8 + ((lane >> 4) & 1) * 4;
#pragma unroll
      for (int i = 0; i < 4; ++i) *(LAS unsigned short*)(sc + 4096 + (d0 + i) * 32 + e * 2) = (unsigned short)(pk2(kv[i], 0.f) & 0xffffu);
    }
    if (w == 0 && lane < 4) {
      u32x2 zv = {pk2(zz[0], zz[1]), pk2(zz[2], zz[3])};
      __builtin_amdgcn_raw_buffer_store_b64(zv, rsW, (unsigned)(WS_Z + rh * 32 + lane * 8), 0, 16);
    }
    LGKM0();
    u32x4 af = *(LAS u32x4*)(sc + 4096 + (lane & 15) * 32 + ((lane >> 4) & 1) * 16);
    if (lane >= 32) af = (u32x4){0u, 0u, 0u, 0u};
    const int gq = (lane >> 4) & 1, q4 = (lane & 15) >> 2, p4 = lane & 3;
#pragma unroll
    for (int c = 0; c < 8; ++c) {
      LAS char* a = sc + (8 * gq + q4) * 256 + c * 32 + p4 * 8;
      s16x4 t0 = __builtin_amdgcn_ds_read_tr16_b64_v4i16((LAS s16x4*)a);
      s16x4 t1 = __builtin_amdgcn_ds_read_tr16_b64_v4i16((LAS s16x4*)(a + 4 * 256));
      bf16x8 bfm = __builtin_bit_cast(bf16x8, __builtin_shufflevector(t0, t1, 0, 1, 2, 3, 4, 5, 6, 7));
      f32x4 o = __builtin_amdgcn_mfma_f32_16x16x32_bf16(__builtin_bit_cast(bf16x8, af), bfm, (f32x4){0.f, 0.f, 0.f, 0.f}, 0, 0, 0);
      u32x2 ov = {pk2(o[0], o[1]), pk2(o[2], o[3])};
      const unsigned n = w * 128 + c * 16 + (lane & 15), nr = n & 63u, q = lane >> 4;
      __builtin_amdgcn_raw_buffer_store_b64(ov, rsW, (unsigned)WS_KW + ((n >> 6) * 8 + (rh >> 2)) * 8192u + ((rh >> 1) & 1) * 4096u + nr * 64u + (((2u * (rh & 1) + (q >> 1)) ^ ((nr >> 2) & 3u)) << 4) + (q & 1) * 8u, 0, 16);
    }
    publish(p, cw, C_R, (GAS unsigned*)(ws + WS_RFLAG) + rh * 32, nullptr, lane);
    stamp(p, w == 0 ? 5 : 63);
  }

  {
    f32x16 aa = {};
    {
      const unsigned oA = offH(qa), oB = offH(qb);
      const int zm = lane & 31;
      unsigned zo[4];
#pragma unroll
      for (int t = 0; t < 4; ++t) zo[t] = (zm < 8 && (zm & 3) == t) ? (unsigned)(L_ZT + t * 32 + h * 16) : (unsigned)L_ZERO;
      Frag f0, f1; bf16x8 z0[4], z1[4];
      f32x16 nm[4]; float inv[4];
      auto l1 = [&](Frag& fn, bf16x8 (&zn)[4], int hg, int t) {
        LAS char* const sw = lds + (unsigned)(tc & (RING - 1)) * 8192u + L_RW;
        fn.a[t] = ld_frag1<K_H>(sw, oA, t); fn.b[t] = ld_frag1<K_H>(sw + (L_RA - L_RW), oB, t);
        const unsigned zs = (zm < 8 && (zm & 3) == t) ? (unsigned)hg * 128u : 0u; zn[t] = lds_r(lds + zo[t] + zs);
      };
      auto step = [&](const Frag& f, const bf16x8 (&z)[4], auto FIRST, auto LOAD, Frag& fn, bf16x8 (&zn)[4], int hgn) {
        constexpr bool first = decltype(FIRST)::value, load = decltype(LOAD)::value;
        f32x16 dn;
        auto fm = [&](int j0, int nn) {
          if (!first) {
#pragma unroll
            for (int j = j0; j < j0 + nn; ++j) { float a_ = aa[j & 15]; asm volatile("v_fmac_f32 %0, %1, %2" : "+v"(a_) : "v"(nm[j >> 4][j & 15]), "v"(inv[j >> 4])); aa[j & 15] = a_; }
          }
        };
        auto ll = [&](int t) { if (load) l1(fn, zn, hgn, t); };
        mf0(dn, z[0], f.b[0]); ll(0); SCHED(); fm(0, 8); SCHED();
        mfa(dn, z[1], f.b[1]); ll(1); SCHED(); fm(8, 8); SCHED();
        mf0(nm[0], f.a[0], f.b[0]); ll(2); SCHED(); fm(16, 8); SCHED();
        mfa(dn, z[2], f.b[2]); ll(3); SCHED(); fm(24, 8); SCHED();
        mf0(nm[1], f.a[1], f.b[1]); SCHED(); fm(32, 8); SCHED();
        mfa(dn, z[3], f.b[3]); SCHED(); fm(40, 8); SCHED();
        mf0(nm[2], f.a[2], f.b[2]); SCHED(); fm(48, 16); SCHED();
        mf0(nm[3], f.a[3], f.b[3]);
        if (first) asm volatile("s_nop 15" : "+v"(dn)); else asm volatile("s_nop 3" : "+v"(dn));
#pragma unroll
        for (int t = 0; t < 4; ++t) inv[t] = __builtin_amdgcn_rcpf(dn[t] + 1e-6f);
        asm volatile("" : "+v"(inv[0]), "+v"(inv[1]), "+v"(inv[2]), "+v"(inv[3]));
        if (load) ++tc;
      };
      GAS unsigned* const uflag = (GAS unsigned*)(ws + WS_UFLAG) + (jj_ * 32 + ft) * 32;
      if (PUB_U == 0 && !isR) publish(p, cw, C_U, uflag, nullptr, lane);
      BAR(); SCHED();
#pragma unroll
      for (int t = 0; t < 4; ++t) l1(f0, z0, 0, t);
      ++tc;
      SCHED(); LGKM0();
      if (PUB_U == 1 && !isR) publish(p, cw, C_U, uflag, nullptr, lane);
      BAR(); SCHED(); step(f0, z0, std::true_type{}, std::true_type{}, f1, z1, 1); SCHED(); LGKM0();
      if (PUB_U == 2 && !isR) publish(p, cw, C_U, uflag, nullptr, lane);
#pragma unroll 1
      for (int i = 2; i < 8; i += 2) {
        BAR(); SCHED(); step(f1, z1, std::false_type{}, std::true_type{}, f0, z0, i); SCHED(); LGKM0();
        BAR(); SCHED(); step(f0, z0, std::false_type{}, std::true_type{}, f1, z1, i + 1); SCHED(); LGKM0();
      }
      step(f1, z1, std::false_type{}, std::false_type{}, f0, z0, 0);
      asm volatile("s_nop 15\n\ts_nop 7" : "+v"(nm[0]), "+v"(nm[1]), "+v"(nm[2]), "+v"(nm[3]));
#pragma unroll
      for (int t = 0; t < 4; ++t)
#pragma unroll
        for (int k = 0; k < 16; ++k) aa[k] = __builtin_fmaf(nm[t][k], inv[t], aa[k]);
      asm volatile("" : "+v"(aa));
    }
    stamp(p, w == 0 ? 6 : 63);
    const int s = st * 64 + 32 * qb + (lane & 31);
    const float b2 = bflo((unsigned)p.b2[s]);
    f32x16 ay = {};
    float g = 0.f;
    kloop<28, false, K_H, K_T>(ay, lds, tc, offH(qa), offT(qb), [&](int i) {
      if (i == 4) g = __builtin_bit_cast(float, __builtin_amdgcn_raw_buffer_load_b32(rsW, (unsigned)(WS_G + s * 4), 0, 16)); });
    stamp(p, w == 0 ? 8 : 63);
    const float g1 = 1.f - g;
    GAS char* ob = (GAS char*)p.out + (size_t)s * 1024 + (jj_ * 64 + 32 * qa) * 2;
#pragma unroll
    for (int kp = 0; kp < 2; ++kp) {
      float v[8];
#pragma unroll
      for (int i = 0; i < 8; ++i) v[i] = g * aa[8 * kp + i] + g1 * (ay[8 * kp + i] + b2);
      unsigned a0 = pk2(v[0], v[1]), a1 = pk2(v[2], v[3]), b0 = pk2(v[4], v[5]), b1 = pk2(v[6], v[7]);
      auto r0 = __builtin_amdgcn_permlane32_swap(a0, b0, false, false);
      auto r1 = __builtin_amdgcn_permlane32_swap(a1, b1, false, false);
      u32x4 o = {r0[0], r1[0], r0[1], r1[1]};
      *(GAS u32x4*)(ob + kp * 32 + h * 16) = o;
    }
    stamp(p, w == 0 ? 10 : 63);
  }
}

extern "C" void kernel_launch(void* const* d_in, const int* in_sizes, int n_in, void* d_out, int out_size, void* d_ws, size_t ws_size,
                              hipStream_t stream) {
  Params p;
  p.x = (const unsigned short*)d_in[0]; p.wqkv = (const unsigned short*)d_in[1]; p.wo = (const unsigned short*)d_in[2];
  p.w1 = (const unsigned short*)d_in[4]; p.b1 = (const unsigned short*)d_in[5]; p.w2 = (const unsigned short*)d_in[6];
  p.b2 = (const unsigned short*)d_in[7]; p.gw = (const unsigned short*)d_in[8];
  p.out = (unsigned short*)d_out; p.ws = (char*)d_ws;
  (void)hipFuncSetAttribute((const void*)fused_k, hipFuncAttributeMaxDynamicSharedMemorySize, L_BYTES);
  fused_k<<<256, 448 + 64 * PROF, L_BYTES, stream>>>(p);
}
```
